```python
import torch

def _softmax(x, axis=-1):
    return torch.softmax(x, dim=axis)
def _gelu(x):
    return torch.nn.functional.gelu(x, approximate="tanh")
def _silu(x):
    return torch.nn.functional.silu(x)
def _sigmoid(x):
    return torch.sigmoid(x)
def _layernorm(x, axis=-1, eps=1e-5):
    m = x.mean(dim=axis, keepdim=True)
    v = x.var(dim=axis, keepdim=True, unbiased=False)
    return (x - m) / torch.sqrt(v + eps)
def _scatter_add(tgt, idx, src, axis=0):
    return tgt.clone().index_add_(axis, idx, src)
def _segment_sum(src, seg, n):
    out = torch.zeros((n,) + src.shape[1:], dtype=src.dtype)
    return out.index_add_(0, seg, src)
def _pad(x, pw):
    flat = tuple(v for pair in reversed(pw) for v in pair)
    return torch.nn.functional.pad(x, flat)

kernel_name = 'parallel_sum__ssm_conv_h3__glu_gated_swiglu__prompt'

def setup_inputs():

    _w = (lambda a: (lambda t: t.to(torch.bfloat16) if t.is_floating_point() else t)(torch.as_tensor(a)))
    def _A_setup():
        B, S, D = 1, 2048, 1024
        ks = 4
        return {
            "q": _w((lambda *s: torch.randn(*s, dtype=torch.bfloat16) * 0.05)(B, S, D)),
            "k": _w((lambda *s: torch.randn(*s, dtype=torch.bfloat16) * 0.2)(B, S, D)),
            "v": _w((lambda *s: torch.randn(*s, dtype=torch.bfloat16) * 0.2)(B, S, D)),
            "shift_k": _w((lambda *s: torch.randn(*s, dtype=torch.bfloat16) * 0.2)(D, ks) * 0.5),
            "ssm_k": _w((lambda *s: torch.randn(*s, dtype=torch.bfloat16) * 0.4)(D, S) * (1.0 / S ** 0.5)),
            "ssm_D": _w((lambda *s: torch.randn(*s, dtype=torch.bfloat16) * 0.2)(D)),
        }
    _A_in = _A_setup()

    def _B_setup():
        B, S, D, F = 1, 2048, 1024, 3584
        return {
            "x": _w((lambda *s: torch.randn(*s, dtype=torch.bfloat16) * 0.05)(B, S, D)),
            "w1": _w((lambda *s: torch.randn(*s, dtype=torch.bfloat16) * 0.2)(D, F) * (2.0 / D) ** 0.5),
            "w3": _w((lambda *s: torch.randn(*s, dtype=torch.bfloat16) * 0.2)(D, F) * (2.0 / D) ** 0.5),
            "w2": _w((lambda *s: torch.randn(*s, dtype=torch.bfloat16) * 0.2)(F, D) * (2.0 / F) ** 0.5),
        }
    _B_in = _B_setup()

    return {
        "A_q": _A_in["q"],
        "A_k": _A_in["k"],
        "A_v": _A_in["v"],
        "A_shift_k": _A_in["shift_k"],
        "A_ssm_k": _A_in["ssm_k"],
        "A_ssm_D": _A_in["ssm_D"],
        "B_x": _B_in["x"],
        "B_w1": _B_in["w1"],
        "B_w3": _B_in["w3"],
        "B_w2": _B_in["w2"],
    }

def reference(A_q, A_k, A_v, A_shift_k, A_ssm_k, A_ssm_D, B_x, B_w1, B_w3, B_w2):
    def _A_ref(q, k, v, shift_k, ssm_k, ssm_D):
        B, S, D = q.shape
        ks = shift_k.shape[-1]
        k_pad = _pad(k, ((0, 0), (ks - 1, 0), (0, 0)))
        k_shift = torch.zeros_like(k)
        for t in range(ks):
            k_shift = k_shift + k_pad[:, t:t + S, :] * shift_k[:, ks - 1 - t][None, None, :]
        kv = k_shift * v
        L = 2 * S
        kv_pad = _pad(kv, ((0, 0), (0, S), (0, 0)))
        kf_pad = _pad(ssm_k, ((0, 0), (0, S)))
        kv_f = (lambda x, axes=(-2,-1): torch.fft.rfft2(x.float(), dim=axes))(kv_pad[:, None, :, :], axes=(1, 2))
        kf_f = (lambda x, axes=(-2,-1): torch.fft.rfft2(x.float(), dim=axes))(kf_pad[:, None, :], axes=(1, 2))
        kf_f = (lambda x, *d: x.permute(*d))(kf_f, 1, 2, 0)
        y_f = kv_f * kf_f[None, :, :, :]
        y = (lambda x, s, axes=(-2,-1): torch.fft.irfft2(x, s=s, dim=axes).to(torch.bfloat16))(y_f, (1, L), axes=(1, 2))[:, 0, :S, :]
        y = y + kv * ssm_D[None, None, :]
        return q * y

    def _B_ref(x, w1, w3, w2):
        gate = torch.einsum("bsd,df->bsf", x, w1)
        up = torch.einsum("bsd,df->bsf", x, w3)
        h = _silu(gate) * up
        out = torch.einsum("bsf,fd->bsd", h, w2)
        return out

    x = A_q
    xn = _layernorm(x, axis=-1)
    return x + _A_ref(xn, A_k, A_v, A_shift_k, A_ssm_k, A_ssm_D) + _B_ref(xn, B_w1, B_w3, B_w2)
```

```cpp
#include <hip/hip_runtime.h>
#include <hip/hip_bf16.h>
#include <type_traits>

typedef unsigned short u16;
typedef short bf16x8 __attribute__((ext_vector_type(8)));
typedef short s16x4 __attribute__((ext_vector_type(4)));
typedef float f32x4 __attribute__((ext_vector_type(4)));
typedef float f32x2 __attribute__((ext_vector_type(2)));
typedef unsigned u32x4 __attribute__((ext_vector_type(4)));
typedef unsigned u32x2 __attribute__((ext_vector_type(2)));
#define LAS __attribute__((address_space(3)))

constexpr int S_ = 2048, D_ = 1024, F_ = 3584;

__device__ __forceinline__ unsigned pk_bf16(float lo, float hi) {
  typedef __bf16 bf2 __attribute__((ext_vector_type(2)));
  f32x2 v = {lo, hi};
  bf2 r = __builtin_convertvector(v, bf2);
  return __builtin_bit_cast(unsigned, r);
}
__device__ __forceinline__ float bf_lo(unsigned u) { return __uint_as_float(u << 16); }
__device__ __forceinline__ float bf_hi(unsigned u) { return __uint_as_float(u & 0xffff0000u); }
__device__ __forceinline__ float rbf(float x) { return bf_lo(pk_bf16(x, x)); }

#define WAIT_V(n) asm volatile("s_waitcnt vmcnt(" #n ")" ::: "memory")
#define WAIT_L(n) asm volatile("s_waitcnt lgkmcnt(" #n ")" ::: "memory")
#define BAR asm volatile("s_barrier" ::: "memory")
#define SCHED __builtin_amdgcn_sched_barrier(0)
template <int N> __device__ __forceinline__ void wait_v() { asm volatile("s_waitcnt vmcnt(%0)" ::"n"(N) : "memory"); }

__device__ __forceinline__ void glds1(const void* g, unsigned vo, unsigned l) {
  asm volatile("s_mov_b32 m0, %2\n\ts_nop 0\n\tglobal_load_lds_dwordx4 %0, %1" ::"v"(vo), "s"(g), "s"(l) : "memory");
}
__device__ __forceinline__ void glds2(const void* g, unsigned vo, unsigned l) {
  asm volatile("s_mov_b32 m0, %2\n\ts_nop 0\n\tglobal_load_lds_dwordx4 %0, %1\n\tglobal_load_lds_dwordx4 %0, %1 offset:1024" ::"v"(vo), "s"(g), "s"(l) : "memory");
}
__device__ __forceinline__ void glds2_56(const void* g0, const void* g1, unsigned vo, unsigned l0, unsigned l1) {
  asm volatile("s_mov_b32 m0, %3\n\ts_mov_b32 exec_hi, 0x00ffffff\n\tglobal_load_lds_dwordx4 %0, %1\n\t"
               "s_mov_b32 m0, %4\n\ts_nop 0\n\tglobal_load_lds_dwordx4 %0, %2\n\ts_mov_b32 exec_hi, -1"
               ::"v"(vo), "s"(g0), "s"(g1), "s"(l0), "s"(l1) : "memory");
}
__device__ __forceinline__ s16x4 tr_read(LAS unsigned char* p) {
  return __builtin_amdgcn_ds_read_tr16_b64_v4i16((LAS s16x4*)p);
}

__global__ __launch_bounds__(256) void k_prep(const u16* __restrict__ q, const u16* __restrict__ k, const u16* __restrict__ v,
                                              const u16* __restrict__ shift_k, u16* __restrict__ xnF, float* __restrict__ stats,
                                              u16* __restrict__ kvT) {
  __shared__ f32x2 red[4][8];
  const int tid = threadIdx.x, lane = tid & 63, wid = tid >> 6;
  if (blockIdx.x < 256) {
    const int bs = blockIdx.x >> 4, bd = blockIdx.x & 15;
    const int d0 = bd * 64 + (lane & 7) * 8;
    const int s0 = bs * 128 + wid * 32 + (lane >> 3) * 4;
    u32x4 kr[7], vr[4];
#pragma unroll
    for (int i = 0; i < 7; ++i) {
      const int s = s0 - 3 + i;
      kr[i] = (s >= 0) ? *(const u32x4*)(k + (size_t)s * D_ + d0) : (u32x4){0u, 0u, 0u, 0u};
    }
#pragma unroll
    for (int i = 0; i < 4; ++i) vr[i] = *(const u32x4*)(v + (size_t)(s0 + i) * D_ + d0);
    u32x4 sk[4];
#pragma unroll
    for (int i = 0; i < 4; ++i) sk[i] = *(const u32x4*)(shift_k + (size_t)d0 * 4 + i * 8);
#pragma unroll
    for (int j = 0; j < 8; ++j) {
      const unsigned w01 = sk[j >> 1][(j & 1) * 2], w23 = sk[j >> 1][(j & 1) * 2 + 1];
      const float w0 = bf_lo(w01), w1 = bf_hi(w01), w2 = bf_lo(w23), w3 = bf_hi(w23);
      float kk[7];
#pragma unroll
      for (int i = 0; i < 7; ++i) kk[i] = (j & 1) ? bf_hi(kr[i][j >> 1]) : bf_lo(kr[i][j >> 1]);
      float o[4];
#pragma unroll
      for (int i = 0; i < 4; ++i) {
        float a = rbf(kk[i] * w3);
        a = rbf(a + rbf(kk[i + 1] * w2));
        a = rbf(a + rbf(kk[i + 2] * w1));
        a = rbf(a + rbf(kk[i + 3] * w0));
        const float vv = (j & 1) ? bf_hi(vr[i][j >> 1]) : bf_lo(vr[i][j >> 1]);
        o[i] = a * vv;
      }
      u32x2 w;
      w.x = pk_bf16(o[3], o[2]);
      w.y = pk_bf16(o[1], o[0]);
      const int pos = (s0 & ~7) + (4 - (s0 & 4));
      *(u32x2*)(kvT + (size_t)(d0 + j) * S_ + pos) = w;
    }
  } else {
    const int row = (blockIdx.x - 256) * 8 + (lane & 7);
    const int part = lane >> 3;
    u32x4 xr[4];
#pragma unroll
    for (int i = 0; i < 4; ++i) xr[i] = *(const u32x4*)(q + (size_t)row * D_ + (wid * 32 + i * 8 + part) * 8);
    float x[32];
#pragma unroll
    for (int i = 0; i < 4; ++i)
#pragma unroll
      for (int j = 0; j < 4; ++j) { x[i * 8 + 2 * j] = bf_lo(xr[i][j]); x[i * 8 + 2 * j + 1] = bf_hi(xr[i][j]); }
    float s = 0.f;
#pragma unroll
    for (int i = 0; i < 32; ++i) s += x[i];
    s += __shfl_xor(s, 8); s += __shfl_xor(s, 16); s += __shfl_xor(s, 32);
    const float mw = s * (1.0f / 256.0f);
    float m2 = 0.f;
#pragma unroll
    for (int i = 0; i < 32; ++i) { const float d = x[i] - mw; m2 += d * d; }
    m2 += __shfl_xor(m2, 8); m2 += __shfl_xor(m2, 16); m2 += __shfl_xor(m2, 32);
    if (lane < 8) red[wid][lane] = (f32x2){mw, m2};
    __syncthreads();
    const f32x2 r0 = red[0][lane & 7], r1 = red[1][lane & 7], r2 = red[2][lane & 7], r3 = red[3][lane & 7];
    const float mean = (r0.x + r1.x + r2.x + r3.x) * 0.25f;
    const float e0 = r0.x - mean, e1 = r1.x - mean, e2 = r2.x - mean, e3 = r3.x - mean;
    const float var = ((r0.y + r1.y) + (r2.y + r3.y) + 256.0f * ((e0 * e0 + e1 * e1) + (e2 * e2 + e3 * e3))) * (1.0f / 1024.0f);
    const float mb = rbf(mean);
    const float sd = rbf(sqrtf(rbf(rbf(var) + 1e-5f)));
    const float rs = 1.0f / sd;
    if (wid == 0 && lane < 8) *(f32x2*)(stats + 2 * row) = (f32x2){mb, rs};
#pragma unroll
    for (int i = 0; i < 4; ++i) {
      u32x4 w;
#pragma unroll
      for (int j = 0; j < 4; ++j) {
        const unsigned c = pk_bf16(x[i * 8 + 2 * j] - mb, x[i * 8 + 2 * j + 1] - mb);
        w[j] = pk_bf16(bf_lo(c) * rs, bf_hi(c) * rs);
      }
      const int c = wid * 32 + i * 8 + part;
      const size_t off = ((((size_t)(row >> 8) * 32 + (c >> 2)) * 16 + ((row >> 4) & 15)) * 64 + (c & 3) * 16 + (row & 15)) * 8;
      *(u32x4*)(xnF + off) = w;
    }
  }
}

constexpr int CV_CS = 4384;
constexpr int CV_CH = 8 * CV_CS + 4352;
__device__ __forceinline__ void conv_body(LAS unsigned char* lds, int ch0, const u16* __restrict__ ssm_k, const u16* __restrict__ kvT,
                                          const u16* __restrict__ ssm_D, u16* __restrict__ y2T) {
  const int tid = threadIdx.x, lane = tid & 63, wid = __builtin_amdgcn_readfirstlane(tid >> 6);
  {
    u32x4 h0[2], h1[2], uu[2];
#pragma unroll
    for (int i = 0; i < 2; ++i) {
      const int c = (tid >> 8) + 2 * i, n = tid & 255;
      const u16* hp = ssm_k + (size_t)(ch0 + c) * S_ + n * 8;
      h1[i] = *(const u32x4*)hp;
      h0[i] = n > 0 ? *(const u32x4*)(hp - 8) : (u32x4){0u, 0u, 0u, 0u};
      uu[i] = *(const u32x4*)(kvT + (size_t)(ch0 + c) * S_ + n * 8);
    }
    {
      const int c = tid >> 7, e = (tid >> 4) & 7, p = tid & 15;
      *(LAS u32x4*)(lds + c * CV_CH + e * CV_CS + p * 16) = (u32x4){0u, 0u, 0u, 0u};
      if (tid < 64) *(LAS u32x4*)(lds + (tid >> 4) * CV_CH + 8 * CV_CS + p * 16) = (u32x4){0u, 0u, 0u, 0u};
    }
#pragma unroll
    for (int i = 0; i < 2; ++i) {
      const int c = (tid >> 8) + 2 * i, n = tid & 255;
      unsigned d[8] = {h0[i][0], h0[i][1], h0[i][2], h0[i][3], h1[i][0], h1[i][1], h1[i][2], h1[i][3]};
      unsigned w[7];
#pragma unroll
      for (int j = 0; j < 7; ++j) w[j] = __builtin_amdgcn_alignbit(d[j + 1], d[j], 16);
      LAS unsigned char* base = lds + c * CV_CH + (n + 16) * 16;
#pragma unroll
      for (int e = 0; e < 8; ++e) {
        const int o = e + 1;
        u32x4 t;
        if (o & 1) { const int b = (o - 1) / 2; t = (u32x4){w[b], w[b + 1], w[b + 2], w[b + 3]}; }
        else { const int b = o / 2; t = (u32x4){d[b], d[b + 1], d[b + 2], d[b + 3]}; }
        *(LAS u32x4*)(base + e * CV_CS) = t;
      }
      *(LAS u32x4*)(base + 8 * CV_CS) = uu[i];
    }
  }
  __syncthreads();
  const int c = wid & 3, half = wid >> 2;
  const int fr = lane & 15, g = lane >> 4;
  LAS unsigned char* cb = lds + c * CV_CH;
  LAS unsigned char* ap = cb + (fr & 7) * CV_CS + (fr >> 3) * 256 + g * 16;
  LAS unsigned char* bp = cb + 8 * CV_CS + (32 + fr - g) * 16;
  f32x4 acc[8];
#pragma unroll
  for (int t = 0; t < 8; ++t) acc[t] = (f32x4){0.f, 0.f, 0.f, 0.f};
#pragma unroll
  for (int bb = 0; bb < 4; ++bb) {
    const int b = half * 4 + bb;
    bf16x8 A[8], B[8];
#pragma unroll
    for (int a = 0; a < 8; ++a) {
      A[a] = *(LAS bf16x8*)(ap + b * 64 + a * 512);
      B[a] = *(LAS bf16x8*)(bp - b * 64 + a * 512);
    }
#pragma unroll
    for (int a = 0; a < 8; ++a)
#pragma unroll
      for (int t = a; t < 8; ++t) acc[t] = __builtin_amdgcn_mfma_f32_16x16x32_bf16(A[a], B[t - a], acc[t], 0, 0, 0);
  }
  __syncthreads();
  {
    LAS unsigned char* xb = cb + half * 4096 + lane * 16;
#pragma unroll
    for (int t = 0; t < 4; ++t) *(LAS f32x4*)(xb + t * 1024) = half ? acc[t] : acc[4 + t];
  }
  __syncthreads();
  {
    LAS unsigned char* xb = cb + (half ^ 1) * 4096 + lane * 16;
    const float Dv = bf_lo((unsigned)ssm_D[ch0 + c]);
#pragma unroll
    for (int t = 0; t < 4; ++t) {
      const f32x4 o = *(LAS f32x4*)(xb + t * 1024);
      const f32x4 m = half ? acc[4 + t] : acc[t];
      const int T = half * 4 + t;
      const int blk = 32 * T + 16 * (g >> 1) + fr;
      const u32x2 ur = *(LAS u32x2*)(cb + 8 * CV_CS + (blk + 16) * 16 + ((g & 1) ? 0 : 8));
      const float u0 = bf_hi(ur.y), u1 = bf_lo(ur.y), u2 = bf_hi(ur.x), u3 = bf_lo(ur.x);
      const float y0 = rbf(m[0] + o[0]) + rbf(u0 * Dv), y1 = rbf(m[1] + o[1]) + rbf(u1 * Dv);
      const float y2 = rbf(m[2] + o[2]) + rbf(u2 * Dv), y3 = rbf(m[3] + o[3]) + rbf(u3 * Dv);
      u32x2 w;
      w.x = pk_bf16(y0, y1);
      w.y = pk_bf16(y2, y3);
      *(u32x2*)(y2T + (size_t)(ch0 + c) * S_ + blk * 8 + 4 * (g & 1)) = w;
    }
  }
}
__global__ __launch_bounds__(512, 2) void k_conv(const u16* __restrict__ ssm_k, const u16* __restrict__ kvT, const u16* __restrict__ ssm_D,
                                                 u16* __restrict__ y2T) {
  __shared__ __attribute__((aligned(16))) unsigned char smem[4 * CV_CH];
  conv_body((LAS unsigned char*)smem, blockIdx.x * 4, ssm_k, kvT, ssm_D, y2T);
}

constexpr int G1_UA = 16384, G1_UPOFF = 7168 + 16, G1_UNIT = G1_UA + 2 * 7168 + 16, G1_NU = 5, G1_D = 3, G1_NK = D_ / 32;

__global__ __launch_bounds__(512, 2) void k_gemm1(const u16* __restrict__ xnF, const u16* __restrict__ w1, const u16* __restrict__ w3,
                                                  u16* __restrict__ hF) {
  __shared__ __attribute__((aligned(16))) unsigned char smem[G1_NU * G1_UNIT];
  LAS unsigned char* lds = (LAS unsigned char*)smem;
  const int tid = threadIdx.x, lane = tid & 63, wid = __builtin_amdgcn_readfirstlane(tid >> 6);
  const unsigned ldsb = (unsigned)(size_t)lds;
  const int wr = wid & 3, wc = wid >> 2;
  const int xcd = blockIdx.x & 7, idx = blockIdx.x >> 3;
  const int pm = idx & 7, pn = xcd * 4 + (idx >> 3);
  const int fr = lane & 15, fq = lane >> 4;
  const char* gA = (const char*)xnF + (size_t)pm * (32 * 16384);
  const unsigned voA = (unsigned)(wid * 2048 + lane * 16);
  const int Q = (wid & 4) | ((wid & 1) << 1) | ((wid >> 1) & 1);
  const int lrow = lane / 14, lch = lane - lrow * 14;
  const unsigned voB = (unsigned)((Q * 4 + lrow) * (F_ * 2) + pn * 224 + lch * 16);
  const char* gG = (const char*)w1;
  const char* gU = (const char*)w3;
  auto stage = [&](int u, int slot) {
    const unsigned sb = ldsb + slot * G1_UNIT;
    glds2(gA + (size_t)u * 16384, voA, sb + wid * 2048);
    glds2_56(gG + (size_t)u * (32 * F_ * 2), gU + (size_t)u * (32 * F_ * 2), voB, sb + G1_UA + wid * 896, sb + G1_UA + G1_UPOFF + wid * 896);
  };
  const int aoff = wr * 4096 + lane * 16;
  const int tq = fr >> 2, tp = lane & 3;
  const int boff = G1_UA + (tp >> 1) * G1_UPOFF + 896 * (4 * (fq >> 1) + (fq & 1)) + 224 * tq + 16 * (7 * wc) + 8 * (tp & 1);
  f32x4 acc[4][7];
#pragma unroll
  for (int m = 0; m < 4; ++m)
#pragma unroll
    for (int n = 0; n < 7; ++n) acc[m][n] = (f32x4){0.f, 0.f, 0.f, 0.f};

#pragma unroll
  for (int u = 0; u < G1_D; ++u) stage(u, u);
  wait_v<(G1_D - 1) * 4>();
  BAR;
  if (wc == 1) BAR;
  int rslot = 0, wslot = G1_D;
  auto body = [&](int u, auto LOADC, auto VMC) {
    constexpr bool LOAD = decltype(LOADC)::value;
    constexpr int VM = decltype(VMC)::value;
    LAS unsigned char* sb = lds + rslot * G1_UNIT;
    bf16x8 A[4], B[7];
#pragma unroll
    for (int n = 0; n < 7; ++n) {
      const s16x4 lo = tr_read(sb + boff + n * 16), hi = tr_read(sb + boff + n * 16 + 2 * 896);
      B[n] = __builtin_shufflevector(lo, hi, 0, 1, 2, 3, 4, 5, 6, 7);
    }
#pragma unroll
    for (int m = 0; m < 4; ++m) A[m] = *(LAS bf16x8*)(sb + aoff + m * 1024);
    if (LOAD) stage(u + G1_D, wslot);
    wait_v<VM>();
    BAR;
    WAIT_L(0);
    SCHED;
    __builtin_amdgcn_s_setprio(1);
#pragma unroll
    for (int n = 0; n < 7; ++n)
#pragma unroll
      for (int m = 0; m < 4; ++m) acc[m][n] = __builtin_amdgcn_mfma_f32_16x16x32_bf16(B[n], A[m], acc[m][n], 0, 0, 0);
    __builtin_amdgcn_s_setprio(0);
    SCHED;
    BAR;
    rslot = (rslot == G1_NU - 1) ? 0 : rslot + 1;
    wslot = (wslot == G1_NU - 1) ? 0 : wslot + 1;
  };
  for (int u = 0; u < G1_NK - G1_D; ++u) body(u, std::true_type{}, std::integral_constant<int, (G1_D - 1) * 4>{});
  body(G1_NK - 3, std::false_type{}, std::integral_constant<int, 4>{});
  body(G1_NK - 2, std::false_type{}, std::integral_constant<int, 0>{});
  body(G1_NK - 1, std::false_type{}, std::integral_constant<int, 0>{});
  if (wc == 0) BAR;

  auto finish = [&](f32x4 a, f32x4 b, int row, int f) {
    float hh[4];
#pragma unroll
    for (int j = 0; j < 4; ++j) {
      const float gt = rbf(a[j]), up = rbf(b[j]);
      const float sg = __builtin_amdgcn_rcpf(1.0f + __builtin_amdgcn_exp2f(gt * -1.44269504089f));
      hh[j] = rbf(gt * sg) * up;
    }
    u32x2 w;
    w.x = pk_bf16(hh[0], hh[1]);
    w.y = pk_bf16(hh[2], hh[3]);
    const size_t off = (((size_t)(row >> 4) * (F_ / 32) + (f >> 5)) * 64 + ((f >> 3) & 3) * 16 + (row & 15)) * 8 + (f & 4);
    *(u32x2*)(hF + off) = w;
  };
  auto swap4 = [&](f32x4& a, f32x4& b) {
#pragma unroll
    for (int j = 0; j < 4; ++j) {
      auto r = __builtin_amdgcn_permlane32_swap(__float_as_uint(a[j]), __float_as_uint(b[j]), false, false);
      a[j] = __uint_as_float(r[0]);
      b[j] = __uint_as_float(r[1]);
    }
  };
  const int row0 = pm * 256 + wr * 64 + fr, fb = pn * 112 + wc * 56;
#pragma unroll
  for (int m = 0; m < 4; ++m) {
#pragma unroll
    for (int n = 0; n < 6; n += 2) {
      f32x4 a = acc[m][n], b = acc[m][n + 1];
      swap4(a, b);
      finish(a, b, row0 + m * 16, fb + n * 8 + 4 * fq);
    }
  }
#pragma unroll
  for (int m = 0; m < 4; m += 2) {
    f32x4 a = acc[m][6], b = acc[m + 1][6];
    swap4(a, b);
    finish(a, b, row0 + (m + (fq >> 1)) * 16, fb + 48 + 4 * (fq & 1));
  }
}

constexpr int G2_UNIT = 24576, G2_NU = 5, G2_D = G2_NU - 1, G2_NK = F_ / 64, G2_YOFF = G2_NU * G2_UNIT;

__global__ __launch_bounds__(512, 2) void k_gemm2(const u16* __restrict__ hF, const u16* __restrict__ w2, const u16* __restrict__ x,
                                                  const float* __restrict__ stats, const u16* __restrict__ y2T, u16* __restrict__ out) {
  __shared__ __attribute__((aligned(16))) unsigned char smem[G2_YOFF + 16384];
  LAS unsigned char* lds = (LAS unsigned char*)smem;
  const int tid = threadIdx.x, lane = tid & 63, wid = __builtin_amdgcn_readfirstlane(tid >> 6);
  const unsigned ldsb = (unsigned)(size_t)lds;
  const int gk = wid >> 2, wr = (wid >> 1) & 1, wc = wid & 1;
  const int xcd = blockIdx.x & 7, idx = blockIdx.x >> 3;
  const int pm = (xcd >> 1) * 4 + (idx & 3), pn = (xcd & 1) * 8 + (idx >> 2);
  const int fr = lane & 15, fq = lane >> 4;
#pragma unroll
  for (int i = 0; i < 2; ++i) {
    const int r = wid * 8 + i * 4 + (lane >> 4);
    glds1((const char*)y2T + ((size_t)(pn * 64) * S_ + pm * 128) * 2, (unsigned)(r * S_ * 2 + (lane & 15) * 16), ldsb + G2_YOFF + (wid * 2 + i) * 1024);
  }
  const char* gA = (const char*)hF + (size_t)(pm * 8 + wid) * (F_ / 32) * 1024;
  const unsigned voA = (unsigned)(lane * 16);
  const int brow = wid * 8 + (lane >> 3);
  const int bswz = ((brow >> 1) & 1) | (((brow >> 3) & 1) << 1);
  const int bch = (lane & 7) ^ (bswz << 1);
  const unsigned voB = (unsigned)(brow * (D_ * 2) + pn * 128 + bch * 16);
  auto stage = [&](int u, int slot) {
    const unsigned sb = ldsb + slot * G2_UNIT;
    glds2(gA + (size_t)u * 2048, voA, sb + wid * 2048);
    glds1((const char*)w2 + (size_t)u * (64 * D_ * 2), voB, sb + 16384 + wid * 1024);
  };
  const int aoff = wr * 8192 + gk * 1024 + lane * 16;
  const int tq = fr >> 2, tp = lane & 3;
  const int fs = (tq >> 1) | ((fq & 1) << 1);
  int boff[2];
#pragma unroll
  for (int n = 0; n < 2; ++n) boff[n] = 16384 + (32 * gk + 8 * fq + tq) * 128 + 32 * ((wc * 2 + n) ^ fs) + 8 * tp;
  f32x4 acc[4][2];
#pragma unroll
  for (int m = 0; m < 4; ++m)
#pragma unroll
    for (int n = 0; n < 2; ++n) acc[m][n] = (f32x4){0.f, 0.f, 0.f, 0.f};
#pragma unroll
  for (int u = 0; u < G2_D; ++u) stage(u, u);
  int rslot = 0, wslot = G2_D;
  auto body = [&](int u, auto LOADC, auto VMC) {
    constexpr bool LOAD = decltype(LOADC)::value;
    constexpr int VM = decltype(VMC)::value;
    wait_v<VM>();
    BAR;
    LAS unsigned char* sb = lds + rslot * G2_UNIT;
    bf16x8 A[4], B[2];
#pragma unroll
    for (int n = 0; n < 2; ++n) {
      const s16x4 lo = tr_read(sb + boff[n]), hi = tr_read(sb + boff[n] + 512);
      B[n] = __builtin_shufflevector(lo, hi, 0, 1, 2, 3, 4, 5, 6, 7);
    }
#pragma unroll
    for (int m = 0; m < 4; ++m) A[m] = *(LAS bf16x8*)(sb + aoff + m * 2048);
    if (LOAD) stage(u + G2_D, wslot);
    WAIT_L(0);
    SCHED;
#pragma unroll
    for (int n = 0; n < 2; ++n)
#pragma unroll
      for (int m = 0; m < 4; ++m) acc[m][n] = __builtin_amdgcn_mfma_f32_16x16x32_bf16(B[n], A[m], acc[m][n], 0, 0, 0);
    SCHED;
    rslot = (rslot == G2_NU - 1) ? 0 : rslot + 1;
    wslot = (wslot == G2_NU - 1) ? 0 : wslot + 1;
  };
  for (int u = 0; u < G2_NK - G2_D; ++u) body(u, std::true_type{}, std::integral_constant<int, (G2_D - 1) * 3>{});
  body(G2_NK - 4, std::false_type{}, std::integral_constant<int, 9>{});
  body(G2_NK - 3, std::false_type{}, std::integral_constant<int, 6>{});
  body(G2_NK - 2, std::false_type{}, std::integral_constant<int, 3>{});
  body(G2_NK - 1, std::false_type{}, std::integral_constant<int, 0>{});
  BAR;
  {
    LAS unsigned char* xb = lds + wid * 4096 + lane * 16;
#pragma unroll
    for (int mm = 0; mm < 2; ++mm)
#pragma unroll
      for (int n = 0; n < 2; ++n) *(LAS f32x4*)(xb + (mm * 2 + n) * 1024) = gk ? acc[mm][n] : acc[2 + mm][n];
  }
  WAIT_L(0);
  BAR;
  {
    LAS unsigned char* xb = lds + (wid ^ 4) * 4096 + lane * 16;
#pragma unroll
    for (int mm = 0; mm < 2; ++mm) {
      const int lrow = wr * 64 + (gk * 2 + mm) * 16;
      const int row = pm * 128 + lrow + fr;
      const f32x2 st = *(const f32x2*)(stats + 2 * row);
#pragma unroll
      for (int n = 0; n < 2; ++n) {
        const f32x4 o = *(LAS f32x4*)(xb + (mm * 2 + n) * 1024);
        const f32x4 mine = gk ? acc[2 + mm][n] : acc[mm][n];
        const int lcol = wc * 32 + n * 16;
        const s16x4 yv = tr_read(lds + G2_YOFF + (lcol + 4 * fq + tq) * 256 + (lrow + 4 * tp) * 2);
        const size_t go = (size_t)row * D_ + pn * 64 + lcol + 4 * fq;
        const u32x2 xv = *(const u32x2*)(x + go);
        const float xf[4] = {bf_lo(xv.x), bf_hi(xv.x), bf_lo(xv.y), bf_hi(xv.y)};
        float r[4];
#pragma unroll
        for (int j = 0; j < 4; ++j) {
          const float xn = rbf(rbf(xf[j] - st.x) * st.y);
          const float yy = __uint_as_float(((unsigned)(unsigned short)yv[j]) << 16);
          const float a = rbf(xn * yy);
          r[j] = rbf(xf[j] + a) + rbf(mine[j] + o[j]);
        }
        u32x2 w;
        w.x = pk_bf16(r[0], r[1]);
        w.y = pk_bf16(r[2], r[3]);
        *(u32x2*)(out + go) = w;
      }
    }
  }
}

extern "C" void kernel_launch(void* const* d_in, const int* in_sizes, int n_in, void* d_out, int out_size, void* d_ws, size_t ws_size,
                              hipStream_t stream) {
  const u16* q = (const u16*)d_in[0];
  const u16* k = (const u16*)d_in[1];
  const u16* v = (const u16*)d_in[2];
  const u16* shift_k = (const u16*)d_in[3];
  const u16* ssm_k = (const u16*)d_in[4];
  const u16* ssm_D = (const u16*)d_in[5];
  const u16* w1 = (const u16*)d_in[7];
  const u16* w3 = (const u16*)d_in[8];
  const u16* w2 = (const u16*)d_in[9];
  char* ws = (char*)d_ws;
  u16* xnF = (u16*)ws;
  u16* kvT = (u16*)(ws + (4u << 20));
  u16* y2T = (u16*)(ws + (8u << 20));
  float* stats = (float*)(ws + (12u << 20));
  u16* hF = (u16*)(ws + (13u << 20));
  k_prep<<<512, 256, 0, stream>>>(q, k, v, shift_k, xnF, stats, kvT);
  k_conv<<<256, 512, 0, stream>>>(ssm_k, kvT, ssm_D, y2T);
  k_gemm1<<<256, 512, 0, stream>>>(xnF, w1, w3, hF);
  k_gemm2<<<256, 512, 0, stream>>>(hF, w2, q, stats, y2T, (u16*)d_out);
}
```

```cpp
#include <hip/hip_runtime.h>
#include <hip/hip_bf16.h>
#include <type_traits>

typedef unsigned short u16;
typedef short bf16x8 __attribute__((ext_vector_type(8)));
typedef short s16x4 __attribute__((ext_vector_type(4)));
typedef float f32x4 __attribute__((ext_vector_type(4)));
typedef float f32x2 __attribute__((ext_vector_type(2)));
typedef unsigned u32x4 __attribute__((ext_vector_type(4)));
typedef unsigned u32x2 __attribute__((ext_vector_type(2)));
#define LAS __attribute__((address_space(3)))
template <int N> using IC = std::integral_constant<int, N>;

constexpr int S_ = 2048, D_ = 1024, F_ = 3584;

__device__ __forceinline__ unsigned pk_bf16(float lo, float hi) {
  typedef __bf16 bf2 __attribute__((ext_vector_type(2)));
  f32x2 v = {lo, hi};
  bf2 r = __builtin_convertvector(v, bf2);
  return __builtin_bit_cast(unsigned, r);
}
__device__ __forceinline__ float bf_lo(unsigned u) { return __uint_as_float(u << 16); }
__device__ __forceinline__ float bf_hi(unsigned u) { return __uint_as_float(u & 0xffff0000u); }
__device__ __forceinline__ float rbf(float x) { return bf_lo(pk_bf16(x, x)); }
__device__ __forceinline__ f32x2 rbf2(f32x2 x) { const unsigned u = pk_bf16(x.x, x.y); return (f32x2){bf_lo(u), bf_hi(u)}; }

#define WAIT_L0 asm volatile("s_waitcnt lgkmcnt(0)" ::: "memory")
#define BAR asm volatile("s_barrier" ::: "memory")
#define SCHED __builtin_amdgcn_sched_barrier(0)
template <int N> __device__ __forceinline__ void wait_v() { asm volatile("s_waitcnt vmcnt(%0)" ::"n"(N) : "memory"); }

#define STAMP_DECL
#define STAMP_AT(i)
#define STAMP_OUT(kid, n)
#define DBG_ARG
#define DBG_PASS

__device__ __forceinline__ void glds1(const void* g, unsigned vo, unsigned l) {
  asm volatile("s_mov_b32 m0, %2\n\ts_nop 0\n\tglobal_load_lds_dwordx4 %0, %1" ::"v"(vo), "s"(g), "s"(l) : "memory");
}
__device__ __forceinline__ void glds2(const void* g, unsigned vo, unsigned l) {
  asm volatile("s_mov_b32 m0, %2\n\ts_nop 0\n\tglobal_load_lds_dwordx4 %0, %1\n\tglobal_load_lds_dwordx4 %0, %1 offset:1024" ::"v"(vo), "s"(g), "s"(l) : "memory");
}
__device__ __forceinline__ void glds2_56(const void* g0, const void* g1, unsigned vo, unsigned l0, unsigned l1) {
  asm volatile("s_mov_b32 m0, %3\n\ts_mov_b32 exec_hi, 0x00ffffff\n\tglobal_load_lds_dwordx4 %0, %1\n\t"
               "s_mov_b32 m0, %4\n\ts_nop 0\n\tglobal_load_lds_dwordx4 %0, %2\n\ts_mov_b32 exec_hi, -1"
               ::"v"(vo), "s"(g0), "s"(g1), "s"(l0), "s"(l1) : "memory");
}
template <int OFF, class T> __device__ __forceinline__ void gload16(T& dst, const void* g, unsigned vo) {
  asm volatile("global_load_dwordx4 %0, %1, %2 offset:%3" : "=v"(dst) : "v"(vo), "s"(g), "n"(OFF) : "memory");
}
__device__ __forceinline__ void touch_line(unsigned& dst, const void* g, unsigned vo) {
  asm volatile("global_load_dword %0, %1, %2" : "=v"(dst) : "v"(vo), "s"(g) : "memory");
}
__device__ __forceinline__ s16x4 tr_read(LAS unsigned char* p) { return __builtin_amdgcn_ds_read_tr16_b64_v4i16((LAS s16x4*)p); }
__device__ __forceinline__ bf16x8 tr_frag(LAS unsigned char* p, int hi_off) {
  const s16x4 lo = tr_read(p), hi = tr_read(p + hi_off);
  return __builtin_shufflevector(lo, hi, 0, 1, 2, 3, 4, 5, 6, 7);
}
#define ST_MODE 0
__device__ __forceinline__ void store8(void* p, u32x2 w) {
  *(u32x2*)p = w;
}

__global__ __launch_bounds__(256) void k_prep(const u16* __restrict__ q, const u16* __restrict__ k, const u16* __restrict__ v,
                                              const u16* __restrict__ shift_k, u16* __restrict__ xnF, float* __restrict__ stats,
                                              u16* __restrict__ kvT DBG_ARG) {
  __shared__ f32x2 red[4][8];
  STAMP_DECL STAMP_AT(0);
  const int tid = threadIdx.x, lane = tid & 63, wid = tid >> 6;
  if (blockIdx.x < 256) {
    const int bs = blockIdx.x >> 4, bd = blockIdx.x & 15;
    const int d0 = bd * 64 + (lane & 7) * 8;
    const int s0 = bs * 128 + wid * 32 + (lane >> 3) * 4;
    u32x4 kr[7], vr[4];
#pragma unroll
    for (int i = 0; i < 7; ++i) {
      const int s = s0 - 3 + i;
      kr[i] = *(const u32x4*)(k + (size_t)(s < 0 ? 0 : s) * D_ + d0);
    }
#pragma unroll
    for (int i = 0; i < 4; ++i) vr[i] = *(const u32x4*)(v + (size_t)(s0 + i) * D_ + d0);
    u32x4 sk[4];
#pragma unroll
    for (int i = 0; i < 4; ++i) sk[i] = *(const u32x4*)(shift_k + (size_t)d0 * 4 + i * 8);
    if (s0 == 0) { kr[0] = (u32x4){0u, 0u, 0u, 0u}; kr[1] = kr[0]; kr[2] = kr[0]; }
    STAMP_AT(1);
#pragma unroll
    for (int jj = 0; jj < 4; ++jj) {
      const f32x2 w0 = {bf_lo(sk[jj][0]), bf_lo(sk[jj][2])}, w1 = {bf_hi(sk[jj][0]), bf_hi(sk[jj][2])};
      const f32x2 w2 = {bf_lo(sk[jj][1]), bf_lo(sk[jj][3])}, w3 = {bf_hi(sk[jj][1]), bf_hi(sk[jj][3])};
      f32x2 kk[7];
#pragma unroll
      for (int i = 0; i < 7; ++i) kk[i] = (f32x2){bf_lo(kr[i][jj]), bf_hi(kr[i][jj])};
      f32x2 o[4];
#pragma unroll
      for (int i = 0; i < 4; ++i) {
        f32x2 a = rbf2(kk[i] * w3);
        a = rbf2(a + rbf2(kk[i + 1] * w2));
        a = rbf2(a + rbf2(kk[i + 2] * w1));
        a = rbf2(a + rbf2(kk[i + 3] * w0));
        o[i] = a * (f32x2){bf_lo(vr[i][jj]), bf_hi(vr[i][jj])};
      }
      const int pos = (s0 & ~7) + (4 - (s0 & 4));
      u32x2 w;
      w.x = pk_bf16(o[3].x, o[2].x); w.y = pk_bf16(o[1].x, o[0].x);
      *(u32x2*)(kvT + (size_t)(d0 + 2 * jj) * S_ + pos) = w;
      w.x = pk_bf16(o[3].y, o[2].y); w.y = pk_bf16(o[1].y, o[0].y);
      *(u32x2*)(kvT + (size_t)(d0 + 2 * jj + 1) * S_ + pos) = w;
    }
    STAMP_AT(2);
    STAMP_AT(3);
    STAMP_OUT(0, 4);
  } else {
    const int row = (blockIdx.x - 256) * 8 + (lane & 7);
    const int part = lane >> 3;
    u32x4 xr[4];
#pragma unroll
    for (int i = 0; i < 4; ++i) xr[i] = *(const u32x4*)(q + (size_t)row * D_ + (wid * 32 + i * 8 + part) * 8);
    f32x2 x[16];
#pragma unroll
    for (int i = 0; i < 4; ++i)
#pragma unroll
      for (int j = 0; j < 4; ++j) x[i * 4 + j] = (f32x2){bf_lo(xr[i][j]), bf_hi(xr[i][j])};
    STAMP_AT(1);
    f32x2 s2 = {0.f, 0.f};
#pragma unroll
    for (int i = 0; i < 16; ++i) s2 += x[i];
    float s = s2.x + s2.y;
    s += __shfl_xor(s, 8); s += __shfl_xor(s, 16); s += __shfl_xor(s, 32);
    const float mw = s * (1.0f / 256.0f);
    f32x2 q2 = {0.f, 0.f};
#pragma unroll
    for (int i = 0; i < 16; ++i) { const f32x2 d = x[i] - mw; q2 += d * d; }
    float m2 = q2.x + q2.y;
    m2 += __shfl_xor(m2, 8); m2 += __shfl_xor(m2, 16); m2 += __shfl_xor(m2, 32);
    if (lane < 8) red[wid][lane] = (f32x2){mw, m2};
    __syncthreads();
    const f32x2 r0 = red[0][lane & 7], r1 = red[1][lane & 7], r2 = red[2][lane & 7], r3 = red[3][lane & 7];
    const float mean = (r0.x + r1.x + r2.x + r3.x) * 0.25f;
    const float e0 = r0.x - mean, e1 = r1.x - mean, e2 = r2.x - mean, e3 = r3.x - mean;
    const float var = ((r0.y + r1.y) + (r2.y + r3.y) + 256.0f * ((e0 * e0 + e1 * e1) + (e2 * e2 + e3 * e3))) * (1.0f / 1024.0f);
    const float mb = rbf(mean);
    const float sd = rbf(sqrtf(rbf(rbf(var) + 1e-5f)));
    const float rs = 1.0f / sd;
    if (wid == 0 && lane < 8) *(f32x2*)(stats + 2 * row) = (f32x2){mb, rs};
#pragma unroll
    for (int i = 0; i < 4; ++i) {
      u32x4 w;
#pragma unroll
      for (int j = 0; j < 4; ++j) {
        const f32x2 c = rbf2(x[i * 4 + j] - mb) * rs;
        w[j] = pk_bf16(c.x, c.y);
      }
      const int c = wid * 32 + i * 8 + part;
      const size_t off = ((((size_t)(row >> 8) * 32 + (c >> 2)) * 16 + ((row >> 4) & 15)) * 64 + (c & 3) * 16 + (row & 15)) * 8;
      *(u32x4*)(xnF + off) = w;
    }
    STAMP_AT(2);
    STAMP_AT(3);
    STAMP_OUT(1, 4);
  }
}

constexpr int CV_CS = 4384;
constexpr int CV_CH = 8 * CV_CS + 4352;
constexpr int G1_UA = 16384, G1_UPOFF = 7168 + 16, G1_UNIT = G1_UA + 2 * 7168 + 16, G1_NU = 5, G1_NK = D_ / 32;
#define G1_D 3
constexpr int K2_LDS = 4 * CV_CH > G1_NU * G1_UNIT ? 4 * CV_CH : G1_NU * G1_UNIT;

__global__ __launch_bounds__(512, 2) void k_gemm1(const u16* __restrict__ xnF, const u16* __restrict__ w1, const u16* __restrict__ w3,
                                                  u16* __restrict__ hF, const u16* __restrict__ ssm_k, const u16* __restrict__ kvT,
                                                  const u16* __restrict__ ssm_D, u16* __restrict__ y2T, const u16* __restrict__ w2 DBG_ARG) {
  __shared__ __attribute__((aligned(16))) unsigned char smem[K2_LDS];
  STAMP_DECL STAMP_AT(0);
  LAS unsigned char* lds = (LAS unsigned char*)smem;
  const int tid = threadIdx.x, lane = tid & 63, wid = __builtin_amdgcn_readfirstlane(tid >> 6);
  const unsigned ldsb = (unsigned)(size_t)lds;
  const int wr = wid & 3, wc = wid >> 2;
  const int xcd = blockIdx.x & 7, idx = blockIdx.x >> 3;
  const int pm = idx & 7, pn = xcd * 4 + (idx >> 3);
  const int fr = lane & 15, fq = lane >> 4;
  const char* gA = (const char*)xnF + (size_t)pm * (32 * 16384);
  const unsigned voA = (unsigned)(wid * 2048 + lane * 16);
  const int Q = (wid & 4) | ((wid & 1) << 1) | ((wid >> 1) & 1);
  const int lrow = lane / 14, lch = lane - lrow * 14;
  const unsigned voB = (unsigned)((Q * 4 + lrow) * (F_ * 2) + pn * 224 + lch * 16);
  const char* gG = (const char*)w1;
  const char* gU = (const char*)w3;
  auto stage = [&](int u, int slot) {
    const unsigned sb = ldsb + slot * G1_UNIT;
    glds2(gA + (size_t)u * 16384, voA, sb + wid * 2048);
    glds2_56(gG + (size_t)u * (32 * F_ * 2), gU + (size_t)u * (32 * F_ * 2), voB, sb + G1_UA + wid * 896, sb + G1_UA + G1_UPOFF + wid * 896);
  };
#pragma unroll
  for (int u = 0; u < G1_D; ++u) stage(u, u);
  unsigned junk0, junk1;
  {
    const int l7 = lane < 56 ? lane : 55, r8 = l7 / 7, seg = l7 - r8 * 7;
    touch_line(junk0, (wid & 1) ? gU : gG, (unsigned)(((idx * 4 + (wid >> 1)) * 8 + r8) * (F_ * 2) + xcd * 896 + seg * 128));
  }
  const int aoff = wr * 4096 + lane * 16;
  const int tq = fr >> 2, tp = lane & 3;
  const int brow = G1_UA + 896 * (4 * (fq >> 1) + (fq & 1)) + 224 * tq + 16 * (7 * wc);
  const int boffP = brow + 8 * tp;
  const int boffM = brow + 96 + (tp >> 1) * G1_UPOFF + 8 * (tp & 1);
  f32x4 acc[4][7];
#pragma unroll
  for (int m = 0; m < 4; ++m)
#pragma unroll
    for (int n = 0; n < 7; ++n) acc[m][n] = (f32x4){0.f, 0.f, 0.f, 0.f};
  wait_v<(G1_D - 1) * 4 + 1>();
  BAR;
  STAMP_AT(1);
  if (wc == 1) BAR;
  auto body = [&](int u, auto RS, auto LOADC, auto VMC) {
    constexpr int rslot = decltype(RS)::value, wslot = (rslot + G1_D) % G1_NU;
    constexpr bool LOAD = decltype(LOADC)::value != 0;
    constexpr int VM = decltype(VMC)::value;
    LAS unsigned char* sb = lds + rslot * G1_UNIT;
    bf16x8 A[4], B[7];
    A[0] = *(LAS bf16x8*)(sb + aoff);
#pragma unroll
    for (int i = 0; i < 3; ++i) {
      B[2 * i] = tr_frag(sb + boffP + i * 32, 2 * 896);
      B[2 * i + 1] = tr_frag(sb + boffP + i * 32 + G1_UPOFF, 2 * 896);
    }
    B[6] = tr_frag(sb + boffM, 2 * 896);
#pragma unroll
    for (int m = 1; m < 4; ++m) A[m] = *(LAS bf16x8*)(sb + aoff + m * 1024);
    if (LOAD) stage(u + G1_D, wslot);
    wait_v<VM>();
    if (G1_D == G1_NU - 1) WAIT_L0;
    BAR;
    WAIT_L0;
    SCHED;
    __builtin_amdgcn_s_setprio(1);
#pragma unroll
    for (int m = 0; m < 4; ++m)
#pragma unroll
      for (int nn = 0; nn < 7; ++nn) {
        const int n = (m & 1) ? 6 - nn : nn;
        acc[m][n] = __builtin_amdgcn_mfma_f32_16x16x32_bf16(B[n], A[m], acc[m][n], 0, 0, 0);
      }
    __builtin_amdgcn_s_setprio(0);
    SCHED;
    BAR;
  };
  constexpr int VF = (G1_D - 1) * 4;
  for (int u = 0; u < 25; u += 5) {
    body(u, IC<0>{}, IC<1>{}, IC<VF>{});
    body(u + 1, IC<1>{}, IC<1>{}, IC<VF>{});
    body(u + 2, IC<2>{}, IC<1>{}, IC<VF>{});
    body(u + 3, IC<3>{}, IC<1>{}, IC<VF>{});
    body(u + 4, IC<4>{}, IC<1>{}, IC<VF>{});
  }
  body(25, IC<0>{}, IC<1>{}, IC<VF>{});
  body(26, IC<1>{}, IC<1>{}, IC<VF>{});
  body(27, IC<2>{}, IC<1>{}, IC<VF>{});
  if (G1_D == 3) body(28, IC<3>{}, IC<1>{}, IC<VF>{});
  const int ch0 = blockIdx.x * 4;
  u32x4 h0[2], h1[2], uu[2];
  {
    const int n = tid & 255;
    const unsigned vo = (unsigned)((tid >> 8) * 4096 + n * 16), vp = n > 0 ? vo - 16u : vo;
    const char* hb = (const char*)ssm_k + (size_t)ch0 * 4096;
    const char* ub = (const char*)kvT + (size_t)ch0 * 4096;
    gload16<0>(h1[0], hb, vo); gload16<0>(h0[0], hb, vp); gload16<0>(uu[0], ub, vo);
    gload16<0>(h1[1], hb + 8192, vo); gload16<0>(h0[1], hb + 8192, vp); gload16<0>(uu[1], ub + 8192, vo);
    touch_line(junk1, w2, (unsigned)(((blockIdx.x * 8 + wid) * 28 + (lane < 28 ? lane : 27)) * 128));
  }
  if (G1_D == 4) body(28, IC<3>{}, IC<0>{}, IC<8 + 7>{});
  body(29, IC<4>{}, IC<0>{}, IC<4 + 7>{});
  body(30, IC<0>{}, IC<0>{}, IC<7>{});
  body(31, IC<1>{}, IC<0>{}, IC<7>{});
  if (wc == 0) BAR;
  STAMP_AT(2);

  {
    auto finish = [&](f32x4 g, f32x4 up, int row, int f) {
      f32x4 e = g * -1.44269504089f;
#pragma unroll
      for (int j = 0; j < 4; ++j) e[j] = __builtin_amdgcn_exp2f(e[j]);
      e = e + 1.0f;
#pragma unroll
      for (int j = 0; j < 4; ++j) e[j] = __builtin_amdgcn_rcpf(e[j]);
      const f32x4 hh = (g * e) * up;
      u32x2 w;
      w.x = pk_bf16(hh[0], hh[1]);
      w.y = pk_bf16(hh[2], hh[3]);
      const unsigned off = ((((unsigned)(row >> 4) * (F_ / 32) + (f >> 5)) * 64 + ((f >> 3) & 3) * 16 + (row & 15)) * 8 + (f & 4)) * 2;
      store8((char*)hF + off, w);
    };
    const int row0 = pm * 256 + wr * 64 + fr, fb = pn * 112 + wc * 56;
#pragma unroll
    for (int m = 0; m < 4; ++m)
#pragma unroll
      for (int i = 0; i < 3; ++i) finish(acc[m][2 * i], acc[m][2 * i + 1], row0 + m * 16, fb + i * 16 + 4 * fq);
#pragma unroll
    for (int m = 0; m < 4; m += 2) {
      f32x4 a = acc[m][6], b = acc[m + 1][6];
#pragma unroll
      for (int j = 0; j < 4; ++j) {
        auto r = __builtin_amdgcn_permlane32_swap(__float_as_uint(a[j]), __float_as_uint(b[j]), false, false);
        a[j] = __uint_as_float(r[0]);
        b[j] = __uint_as_float(r[1]);
      }
      finish(a, b, row0 + (m + (fq >> 1)) * 16, fb + 48 + 4 * (fq & 1));
    }
  }
  STAMP_AT(3);
  asm volatile("s_waitcnt vmcnt(15)" : "+v"(h0[0]), "+v"(h0[1]), "+v"(h1[0]), "+v"(h1[1]), "+v"(uu[0]), "+v"(uu[1]) :: "memory");
  {
    {
      const int c = tid >> 7, e = (tid >> 4) & 7, p = tid & 15;
      *(LAS u32x4*)(lds + c * CV_CH + e * CV_CS + p * 16) = (u32x4){0u, 0u, 0u, 0u};
      if (tid < 64) *(LAS u32x4*)(lds + (tid >> 4) * CV_CH + 8 * CV_CS + p * 16) = (u32x4){0u, 0u, 0u, 0u};
    }
#pragma unroll
    for (int i = 0; i < 2; ++i) {
      const int c = (tid >> 8) + 2 * i, n = tid & 255;
      if (n == 0) h0[i] = (u32x4){0u, 0u, 0u, 0u};
      unsigned d[8] = {h0[i][0], h0[i][1], h0[i][2], h0[i][3], h1[i][0], h1[i][1], h1[i][2], h1[i][3]};
      unsigned w[7];
#pragma unroll
      for (int j = 0; j < 7; ++j) w[j] = __builtin_amdgcn_alignbit(d[j + 1], d[j], 16);
      LAS unsigned char* base = lds + c * CV_CH + (n + 16) * 16;
#pragma unroll
      for (int e = 0; e < 8; ++e) {
        const int o = e + 1;
        u32x4 t;
        if (o & 1) { const int b = (o - 1) / 2; t = (u32x4){w[b], w[b + 1], w[b + 2], w[b + 3]}; }
        else { const int b = o / 2; t = (u32x4){d[b], d[b + 1], d[b + 2], d[b + 3]}; }
        *(LAS u32x4*)(base + e * CV_CS) = t;
      }
      *(LAS u32x4*)(base + 8 * CV_CS) = uu[i];
    }
  }
  const float Dv = bf_lo((unsigned)ssm_D[ch0 + (wid & 3)]);
  WAIT_L0;
  BAR;
  STAMP_AT(4);
  {
    const int c = wid & 3, half = wid >> 2;
    LAS unsigned char* cb = lds + c * CV_CH;
    LAS unsigned char* ap = cb + (fr & 7) * CV_CS + (fr >> 3) * 256 + fq * 16 + half * 256;
    LAS unsigned char* bp = cb + 8 * CV_CS + (32 + fr - fq) * 16 - half * 256 - 192;
    f32x4 cacc[8];
#pragma unroll
    for (int t = 0; t < 8; ++t) cacc[t] = (f32x4){0.f, 0.f, 0.f, 0.f};
#pragma unroll
    for (int bb = 0; bb < 4; ++bb) {
      bf16x8 A[8], B[8];
#pragma unroll
      for (int a = 0; a < 8; ++a) {
        A[a] = *(LAS bf16x8*)(ap + bb * 64 + a * 512);
        B[a] = *(LAS bf16x8*)(bp + (3 - bb) * 64 + a * 512);
      }
#pragma unroll
      for (int dl = 0; dl < 8; ++dl)
#pragma unroll
        for (int a = 0; a + dl < 8; ++a) cacc[a + dl] = __builtin_amdgcn_mfma_f32_16x16x32_bf16(A[a], B[dl], cacc[a + dl], 0, 0, 0);
    }
    WAIT_L0;
    BAR;
    STAMP_AT(5);
    {
      LAS unsigned char* xb = cb + half * 4096 + lane * 16;
#pragma unroll
      for (int t = 0; t < 4; ++t) *(LAS f32x4*)(xb + t * 1024) = half ? cacc[t] : cacc[4 + t];
    }
    WAIT_L0;
    BAR;
    LAS unsigned char* xb = cb + (half ^ 1) * 4096 + lane * 16;
#pragma unroll
    for (int t = 0; t < 4; ++t) {
      const f32x4 o = *(LAS f32x4*)(xb + t * 1024);
      const f32x4 m = (half ? cacc[4 + t] : cacc[t]) + o;
      const int blk = 32 * (half * 4 + t) + 16 * (fq >> 1) + fr;
      const u32x2 ur = *(LAS u32x2*)(cb + 8 * CV_CS + (blk + 16) * 16 + ((fq & 1) ? 0 : 8));
      const f32x2 ya = rbf2((f32x2){m[0], m[1]}) + rbf2((f32x2){bf_hi(ur.y), bf_lo(ur.y)} * Dv);
      const f32x2 yb = rbf2((f32x2){m[2], m[3]}) + rbf2((f32x2){bf_hi(ur.x), bf_lo(ur.x)} * Dv);
      u32x2 w;
      w.x = pk_bf16(ya.x, ya.y);
      w.y = pk_bf16(yb.x, yb.y);
      store8(y2T + (size_t)(ch0 + c) * S_ + blk * 8 + 4 * (fq & 1), w);
    }
  }
  STAMP_AT(6);
  wait_v<0>();
  asm volatile("" ::"v"(junk0));
  asm volatile("" ::"v"(junk1));
  STAMP_AT(7);
  STAMP_OUT(2, 8);
}

constexpr int G2_R = 7, G2_NK = F_ / 128, G2_SLOT = 16384, G2_YOFF = G2_R * G2_SLOT;

__global__ __launch_bounds__(512, 2) void k_gemm2(const u16* __restrict__ hF, const u16* __restrict__ w2, const u16* __restrict__ x,
                                                  const float* __restrict__ stats, const u16* __restrict__ y2T, u16* __restrict__ out DBG_ARG) {
  __shared__ __attribute__((aligned(16))) unsigned char smem[G2_YOFF + 16384];
  STAMP_DECL STAMP_AT(0);
  LAS unsigned char* lds = (LAS unsigned char*)smem;
  const int tid = threadIdx.x, lane = tid & 63, wid = __builtin_amdgcn_readfirstlane(tid >> 6);
  const unsigned ldsb = (unsigned)(size_t)lds;
  const int gk = wid >> 2, wr = wid & 3;
  const int xcd = blockIdx.x & 7, idx = blockIdx.x >> 3;
  const int pm = (xcd >> 1) * 4 + (idx & 3), pn = (xcd & 1) * 8 + (idx >> 2);
  const int fr = lane & 15, fq = lane >> 4;
  const char* gA = (const char*)hF + (size_t)(pm * 8 + 2 * wr) * (F_ / 32 * 1024) + gk * 2048;
  const unsigned voA0 = (unsigned)(lane * 16), voA1 = voA0 + (F_ / 32 * 1024);
  unsigned voB[2];
#pragma unroll
  for (int i = 0; i < 2; ++i) {
    const int r = wid * 16 + i * 8 + (lane >> 3);
    const int sw = ((r >> 1) & 1) | (((r >> 3) & 1) << 1);
    voB[i] = (unsigned)(r * (D_ * 2) + pn * 128 + ((lane & 7) ^ (sw << 1)) * 16);
  }
  bf16x8 A[G2_R][2][2];
  auto stage = [&](int u, auto SL) {
    constexpr int sl = decltype(SL)::value;
    const char* ga = gA + (size_t)u * 4096;
    const char* gb = (const char*)w2 + (size_t)u * (128 * D_ * 2);
    glds1(gb, voB[0], ldsb + sl * G2_SLOT + wid * 2048);
    glds1(gb, voB[1], ldsb + sl * G2_SLOT + wid * 2048 + 1024);
    gload16<0>(A[sl][0][0], ga, voA0);
    gload16<0>(A[sl][1][0], ga, voA1);
    gload16<1024>(A[sl][0][1], ga, voA0);
    gload16<1024>(A[sl][1][1], ga, voA1);
  };
  stage(0, IC<0>{}); stage(1, IC<1>{}); stage(2, IC<2>{}); stage(3, IC<3>{}); stage(4, IC<4>{}); stage(5, IC<5>{});
  const int tq = fr >> 2, tp = lane & 3;
  const int fs = (tq >> 1) | ((fq & 1) << 1);
  int boff[4];
#pragma unroll
  for (int n = 0; n < 4; ++n) boff[n] = (64 * gk + 8 * fq + tq) * 128 + 32 * (n ^ fs) + 8 * tp;
  f32x4 acc[2][4];
  bf16x8 X[4], Y[4];
#pragma unroll
  for (int n = 0; n < 4; ++n) {
    acc[0][n] = (f32x4){0.f, 0.f, 0.f, 0.f}; acc[1][n] = acc[0][n];
    Y[n] = (bf16x8){0, 0, 0, 0, 0, 0, 0, 0};
  }
  A[G2_R - 1][0][1] = Y[0]; A[G2_R - 1][1][1] = Y[0];
  auto mma = [&](const bf16x8& a0, const bf16x8& a1, const bf16x8 (&B)[4]) {
#pragma unroll
    for (int n = 0; n < 4; ++n) acc[0][n] = __builtin_amdgcn_mfma_f32_16x16x32_bf16(B[n], a0, acc[0][n], 0, 0, 0);
#pragma unroll
    for (int n = 3; n >= 0; --n) acc[1][n] = __builtin_amdgcn_mfma_f32_16x16x32_bf16(B[n], a1, acc[1][n], 0, 0, 0);
  };
  auto body = [&](int u, auto SL, auto LOADC, auto VMC) {
    constexpr int sl = decltype(SL)::value, pv = (sl + G2_R - 1) % G2_R;
    constexpr bool LOAD = decltype(LOADC)::value != 0;
    constexpr int VM = decltype(VMC)::value;
    asm volatile("s_waitcnt vmcnt(%4) lgkmcnt(0)\n\ts_barrier" : "+v"(A[sl][0][0]), "+v"(A[sl][0][1]), "+v"(A[sl][1][0]), "+v"(A[sl][1][1]) : "n"(VM) : "memory");
    LAS unsigned char* sb = lds + sl * G2_SLOT;
#pragma unroll
    for (int n = 0; n < 4; ++n) X[n] = tr_frag(sb + boff[n], 512);
    SCHED;
    mma(A[pv][0][1], A[pv][1][1], Y);
    SCHED;
#pragma unroll
    for (int n = 0; n < 4; ++n) Y[n] = tr_frag(sb + boff[n] + 4096, 512);
    if (LOAD) stage(u + G2_R - 1, IC<pv>{});
    SCHED;
    mma(A[sl][0][0], A[sl][1][0], X);
    SCHED;
  };
  STAMP_AT(1);
  constexpr int VF = (G2_R - 2) * 6;
  for (int u = 0; u < 21; u += 7) {
    body(u, IC<0>{}, IC<1>{}, IC<VF>{});
    body(u + 1, IC<1>{}, IC<1>{}, IC<VF>{});
    body(u + 2, IC<2>{}, IC<1>{}, IC<VF>{});
    body(u + 3, IC<3>{}, IC<1>{}, IC<VF>{});
    body(u + 4, IC<4>{}, IC<1>{}, IC<VF>{});
    body(u + 5, IC<5>{}, IC<1>{}, IC<VF>{});
    body(u + 6, IC<6>{}, IC<1>{}, IC<VF>{});
  }
  body(21, IC<0>{}, IC<1>{}, IC<VF>{});
  const int lrow = wr * 32 + gk * 16;
  const int row = pm * 128 + lrow + fr;
  u32x2 xv[4];
  f32x2 st;
  {
#pragma unroll
    for (int i = 0; i < 2; ++i) {
      const int r = wid * 8 + i * 4 + (lane >> 4);
      glds1((const char*)y2T + ((size_t)(pn * 64) * S_ + pm * 128) * 2, (unsigned)(r * S_ * 2 + (lane & 15) * 16), ldsb + G2_YOFF + (wid * 2 + i) * 1024);
    }
    const unsigned vx = (unsigned)((row * D_ + pn * 64 + 4 * fq) * 2);
    asm volatile("global_load_dwordx2 %0, %4, %5\n\tglobal_load_dwordx2 %1, %4, %5 offset:32\n\t"
                 "global_load_dwordx2 %2, %4, %5 offset:64\n\tglobal_load_dwordx2 %3, %4, %5 offset:96"
                 : "=&v"(xv[0]), "=&v"(xv[1]), "=&v"(xv[2]), "=&v"(xv[3]) : "v"(vx), "s"(x) : "memory");
    asm volatile("global_load_dwordx2 %0, %1, %2" : "=v"(st) : "v"((unsigned)(row * 8)), "s"(stats) : "memory");
  }
  body(22, IC<1>{}, IC<0>{}, IC<30 + 7>{});
  body(23, IC<2>{}, IC<0>{}, IC<24 + 7>{});
  body(24, IC<3>{}, IC<0>{}, IC<18 + 7>{});
  body(25, IC<4>{}, IC<0>{}, IC<12 + 7>{});
  body(26, IC<5>{}, IC<0>{}, IC<6 + 7>{});
  body(27, IC<6>{}, IC<0>{}, IC<7>{});
  mma(A[6][0][1], A[6][1][1], Y);
  STAMP_AT(2);
  {
    LAS unsigned char* xb = lds + wid * 4096 + lane * 16;
#pragma unroll
    for (int n = 0; n < 4; ++n) *(LAS f32x4*)(xb + n * 1024) = gk ? acc[0][n] : acc[1][n];
  }
  asm volatile("s_waitcnt vmcnt(0) lgkmcnt(0)\n\ts_barrier" : "+v"(xv[0]), "+v"(xv[1]), "+v"(xv[2]), "+v"(xv[3]), "+v"(st) :: "memory");
  {
    LAS unsigned char* xb = lds + (wid ^ 4) * 4096 + lane * 16;
#pragma unroll
    for (int n = 0; n < 4; ++n) {
      const f32x4 b = *(LAS f32x4*)(xb + n * 1024) + (gk ? acc[1][n] : acc[0][n]);
      const u32x2 yv = __builtin_bit_cast(u32x2, tr_read(lds + G2_YOFF + (n * 16 + 4 * fq + tq) * 256 + (lrow + 4 * tp) * 2));
      u32x2 w;
#pragma unroll
      for (int j = 0; j < 2; ++j) {
        const f32x2 xf = {bf_lo(xv[n][j]), bf_hi(xv[n][j])};
        const f32x2 xn = rbf2(rbf2(xf - st.x) * st.y);
        const f32x2 a = rbf2(xn * (f32x2){bf_lo(yv[j]), bf_hi(yv[j])});
        const f32x2 r = rbf2(xf + a) + rbf2((f32x2){b[2 * j], b[2 * j + 1]});
        w[j] = pk_bf16(r.x, r.y);
      }
      *(u32x2*)(out + (size_t)row * D_ + pn * 64 + n * 16 + 4 * fq) = w;
    }
  }
  STAMP_AT(3);
  STAMP_AT(4);
  STAMP_OUT(3, 5);
}

extern "C" void kernel_launch(void* const* d_in, const int* in_sizes, int n_in, void* d_out, int out_size, void* d_ws, size_t ws_size,
                              hipStream_t stream) {
  const u16* q = (const u16*)d_in[0];
  const u16* k = (const u16*)d_in[1];
  const u16* v = (const u16*)d_in[2];
  const u16* shift_k = (const u16*)d_in[3];
  const u16* ssm_k = (const u16*)d_in[4];
  const u16* ssm_D = (const u16*)d_in[5];
  const u16* w1 = (const u16*)d_in[7];
  const u16* w3 = (const u16*)d_in[8];
  const u16* w2 = (const u16*)d_in[9];
  char* ws = (char*)d_ws;
  u16* xnF = (u16*)ws;
  u16* kvT = (u16*)(ws + (4u << 20));
  u16* y2T = (u16*)(ws + (8u << 20));
  float* stats = (float*)(ws + (12u << 20));
  u16* hF = (u16*)(ws + (13u << 20));
  k_prep<<<512, 256, 0, stream>>>(q, k, v, shift_k, xnF, stats, kvT DBG_PASS);
  k_gemm1<<<256, 512, 0, stream>>>(xnF, w1, w3, hF, ssm_k, kvT, ssm_D, y2T, w2 DBG_PASS);
  k_gemm2<<<256, 512, 0, stream>>>(hF, w2, q, stats, y2T, (u16*)d_out DBG_PASS);
}
```

```cpp
#include <hip/hip_runtime.h>
#include <hip/hip_bf16.h>
#include <type_traits>

typedef unsigned short u16;
typedef short bf16x8 __attribute__((ext_vector_type(8)));
typedef short s16x4 __attribute__((ext_vector_type(4)));
typedef float f32x4 __attribute__((ext_vector_type(4)));
typedef float f32x2 __attribute__((ext_vector_type(2)));
typedef unsigned u32x4 __attribute__((ext_vector_type(4)));
typedef unsigned u32x2 __attribute__((ext_vector_type(2)));
#define LAS __attribute__((address_space(3)))
template <int N> using IC = std::integral_constant<int, N>;

constexpr int S_ = 2048, D_ = 1024, F_ = 3584;

constexpr unsigned WS_E = 0, WS_KVF = 1024, WS_HFL = 2048, WS_YFL = 3072, WS_ST = 4096, WS_CS = 32768;
constexpr unsigned WS_KVT = 1u << 20, WS_Y2 = 5u << 20, WS_H = 9u << 20, WS_END = 23u << 20;
constexpr unsigned SPIN_CAP = 1u << 18;

__device__ __forceinline__ unsigned pk_bf16(float lo, float hi) {
  typedef __bf16 bf2 __attribute__((ext_vector_type(2)));
  f32x2 v = {lo, hi};
  bf2 r = __builtin_convertvector(v, bf2);
  return __builtin_bit_cast(unsigned, r);
}
__device__ __forceinline__ float bf_lo(unsigned u) { return __uint_as_float(u << 16); }
__device__ __forceinline__ float bf_hi(unsigned u) { return __uint_as_float(u & 0xffff0000u); }
__device__ __forceinline__ float rbf(float x) { return bf_lo(pk_bf16(x, x)); }
__device__ __forceinline__ f32x2 rbf2(f32x2 x) { const unsigned u = pk_bf16(x.x, x.y); return (f32x2){bf_lo(u), bf_hi(u)}; }

#define WAIT_L0 asm volatile("s_waitcnt lgkmcnt(0)" ::: "memory")
#define BAR asm volatile("s_barrier" ::: "memory")
#define SCHED __builtin_amdgcn_sched_barrier(0)
template <int N> __device__ __forceinline__ void wait_v() { asm volatile("s_waitcnt vmcnt(%0)" ::"n"(N) : "memory"); }

#define STAMP_DECL
#define STAMP_AT(i)
#define STAMP_OUT(n)

__device__ __forceinline__ void glds2v(const void* g, unsigned vo0, unsigned vo1, unsigned l) {
  asm volatile("s_mov_b32 m0, %3\n\ts_nop 0\n\tglobal_load_lds_dwordx4 %0, %2\n\tglobal_load_lds_dwordx4 %1, %2 offset:1024" ::"v"(vo0), "v"(vo1), "s"(g), "s"(l) : "memory");
}
__device__ __forceinline__ void glds1(const void* g, unsigned vo, unsigned l) {
  asm volatile("s_mov_b32 m0, %2\n\ts_nop 0\n\tglobal_load_lds_dwordx4 %0, %1" ::"v"(vo), "s"(g), "s"(l) : "memory");
}
__device__ __forceinline__ void glds1_56(const void* g, unsigned vo, unsigned l) {
  asm volatile("s_mov_b32 m0, %2\n\ts_mov_b32 exec_hi, 0x00ffffff\n\tglobal_load_lds_dwordx4 %0, %1\n\ts_mov_b32 exec_hi, -1" ::"v"(vo), "s"(g), "s"(l) : "memory");
}
template <int OFF, class T> __device__ __forceinline__ void gload16(T& dst, const void* g, unsigned vo) {
  asm volatile("global_load_dwordx4 %0, %1, %2 offset:%3" : "=v"(dst) : "v"(vo), "s"(g), "n"(OFF) : "memory");
}
template <int OFF, class T> __device__ __forceinline__ void gload16s(T& dst, const void* g, unsigned vo) {
  asm volatile("global_load_dwordx4 %0, %1, %2 offset:%3 sc1" : "=v"(dst) : "v"(vo), "s"(g), "n"(OFF) : "memory");
}
template <int OFF, class T> __device__ __forceinline__ void gload8(T& dst, const void* g, unsigned vo) {
  asm volatile("global_load_dwordx2 %0, %1, %2 offset:%3" : "=v"(dst) : "v"(vo), "s"(g), "n"(OFF) : "memory");
}
template <int OFF, class T> __device__ __forceinline__ void gload8s(T& dst, const void* g, unsigned vo) {
  asm volatile("global_load_dwordx2 %0, %1, %2 offset:%3 sc1" : "=v"(dst) : "v"(vo), "s"(g), "n"(OFF) : "memory");
}
__device__ __forceinline__ void gload4s(unsigned& dst, const void* g, unsigned vo) {
  asm volatile("global_load_dword %0, %1, %2 sc1" : "=v"(dst) : "v"(vo), "s"(g) : "memory");
}
__device__ __forceinline__ void touch_line(unsigned& dst, const void* g, unsigned vo) {
  asm volatile("global_load_dword %0, %1, %2" : "=v"(dst) : "v"(vo), "s"(g) : "memory");
}
__device__ __forceinline__ s16x4 tr_read(LAS unsigned char* p) { return __builtin_amdgcn_ds_read_tr16_b64_v4i16((LAS s16x4*)p); }
__device__ __forceinline__ bf16x8 tr_frag(LAS unsigned char* p, int hi_off) {
  const s16x4 lo = tr_read(p), hi = tr_read(p + hi_off);
  return __builtin_shufflevector(lo, hi, 0, 1, 2, 3, 4, 5, 6, 7);
}
template <int CTRL> __device__ __forceinline__ float dpp_add(float x) {
  return x + __uint_as_float(__builtin_amdgcn_update_dpp(0u, __float_as_uint(x), CTRL, 0xf, 0xf, true));
}
__device__ __forceinline__ float wave_sum(float x) {
  x = dpp_add<0xB1>(x); x = dpp_add<0x4E>(x); x = dpp_add<0x141>(x); x = dpp_add<0x140>(x);
  auto r = __builtin_amdgcn_permlane16_swap(__float_as_uint(x), __float_as_uint(x), false, false);
  x = __uint_as_float(r[0]) + __uint_as_float(r[1]);
  auto t = __builtin_amdgcn_permlane32_swap(__float_as_uint(x), __float_as_uint(x), false, false);
  return __uint_as_float(t[0]) + __uint_as_float(t[1]);
}
__device__ __forceinline__ void spin_flag(unsigned& f, const char* base, unsigned vo, unsigned epoch) {
  for (unsigned n = 0; !__all(f == epoch) && n < SPIN_CAP; ++n) {
    __builtin_amdgcn_s_sleep(2);
    asm volatile("global_load_dword %0, %1, %2 sc1\n\ts_waitcnt vmcnt(0)" : "=&v"(f) : "v"(vo), "s"(base) : "memory");
  }
}

template <bool FIRST> __device__ __forceinline__ void mfma1(f32x4& c, const bf16x8& b, const bf16x8& a) {
  if constexpr (FIRST) asm volatile("v_mfma_f32_16x16x32_bf16 %0, %1, %2, 0" : "=&v"(c) : "v"(b), "v"(a));
  else asm volatile("v_mfma_f32_16x16x32_bf16 %0, %1, %2, %0" : "+v"(c) : "v"(b), "v"(a));
}
#define PIN(...) asm volatile("" : __VA_ARGS__)
template <int I, int N, class F> __device__ __forceinline__ void static_for(F&& f) {
  if constexpr (I < N) { f(IC<I>{}); static_for<I + 1, N>(f); }
}
#define KVL 6
#define T0Q 2
#define TW1 1
constexpr int g1_dma(int w) { return (w <= 27 ? 2 : 0) + (w <= 26 ? 2 : 0); }
constexpr int g1_extra(int w) {
  return (w == -1 ? 4 : 0) + (T0Q < 3 && w == TW1 ? 1 : 0) + (w == 4 ? 1 : 0) + (w == KVL ? 4 : w > KVL && w < KVL + 4 ? 3 : 0) + (w == KVL + 9 ? 2 : 0) +
         (w == 16 ? 1 : 0) + (w == 20 ? 1 : 0) + (w == 24 ? 5 : 0) + (w == 26 ? 4 : 0) + (w == 27 ? 2 : 0);
}
__device__ __forceinline__ int stat_pos(int r) { return (r & ~63) | ((r & 15) << 2) | ((r >> 4) & 3); }

constexpr int G1_ASL = 32768, G1_BOFF = 3 * G1_ASL, G1_UPOFF = 7168 + 16, G1_BSL = 2 * 7168 + 16;
constexpr int CV_CS = 4384;
constexpr int CV_CH = 8 * CV_CS + 4352;
constexpr int L_CS = 4 * CV_CH, L_CNT = L_CS + 896;
constexpr int G2_R = 7, G2_NK = F_ / 128, G2_SLOT = 16384;
constexpr int LDS_BYTES = 163840;
static_assert(G1_BOFF + 4 * G1_BSL <= L_CS && L_CNT + 4 <= LDS_BYTES, "lds map");
__host__ __device__ constexpr int g2_slot(int i) { return i < 6 ? 65536 + i * G2_SLOT : 0; }

__global__ __launch_bounds__(512, 2) void k_fused(const u16* __restrict__ x, const u16* __restrict__ kin, const u16* __restrict__ vin,
                                                  const u16* __restrict__ shift_k, const u16* __restrict__ ssm_k, const u16* __restrict__ ssm_D,
                                                  const u16* __restrict__ w1, const u16* __restrict__ w3, const u16* __restrict__ w2,
                                                  u16* __restrict__ out, char* __restrict__ ws) {
  __shared__ __attribute__((aligned(16))) unsigned char smem[LDS_BYTES];
  STAMP_DECL STAMP_AT(0);
  LAS unsigned char* lds = (LAS unsigned char*)smem;
  const int tid = threadIdx.x, lane = tid & 63, wid = __builtin_amdgcn_readfirstlane(tid >> 6);
  const unsigned ldsb = (unsigned)(size_t)lds;
  const int bid = blockIdx.x, xcd = bid & 7, idx = bid >> 3;
  const int fr = lane & 15, fq = lane >> 4, tq = fr >> 2, tp = lane & 3;
  const __amdgpu_buffer_rsrc_t wsr = __builtin_amdgcn_make_buffer_rsrc(ws, 0, WS_END, 0x00020000);

  const int wr = wid & 3, wc = wid >> 2;
  const int pm = idx & 7, pn = xcd * 4 + (idx >> 3);
  const char* gX = (const char*)x + (size_t)pm * (256 * D_ * 2);
  const unsigned voA0 = (unsigned)((wid * 16 + (lane >> 3)) * (D_ * 2) + (((lane & 7) ^ (lane >> 4)) * 16));
  const unsigned voA1 = (unsigned)((wid * 16 + 8 + (lane >> 3)) * (D_ * 2) + (((lane & 7) ^ (4 + (lane >> 4))) * 16));
  const int Q = (wid & 4) | ((wid & 1) << 1) | ((wid >> 1) & 1);
  const int lrow = lane / 14, lch = lane - lrow * 14;
  const unsigned voB = (unsigned)((Q * 4 + lrow) * (F_ * 2) + pn * 224 + lch * 16);
  auto stageA = [&](int j, int half, int piece) {
    glds1(gX + half * (128 * D_ * 2) + ((j + 8 * (pm & 1)) & 15) * 128, piece ? voA1 : voA0, ldsb + (j % 3) * G1_ASL + half * 16384 + wid * 2048 + piece * 1024);
  };
  auto stageB = [&](int u, int mat) {
    glds1_56((const char*)(mat ? w3 : w1) + (size_t)((u + 16 * (pm & 1)) & 31) * (32 * F_ * 2), voB, ldsb + G1_BOFF + (u & 3) * G1_BSL + wid * 896 + mat * G1_UPOFF);
  };
  stageA(0, 0, 0); stageA(0, 0, 1); stageB(0, 0); stageB(0, 1); stageA(0, 1, 0); stageA(0, 1, 1);
  STAMP_AT(17);
  if (tid == 0) *(LAS unsigned*)(lds + L_CNT) = 0u;

  unsigned ev, epoch = 0, junk0, junk1 = 0, junk2;
  u32x4 xr[2], sk[2];
  u32x2 kr[7], vr[4], P[4];
  float fa = 0.f, fb_ = 0.f, st_mean = 0.f, kk[7], tt[4], oo[4];
  const char* gT = (const char*)((wid & 1) ? w3 : w1) + (size_t)(pm & 1) * (512 * F_ * 2);
  auto touch_off = [&](int nq, int q0) {
    const int l = lane < 14 * nq ? lane : 14 * nq - 1, q = l / 14, r = (l - q * 14) / 7, seg = l - q * 14 - r * 7;
    return (unsigned)((128 * (q + q0) + 8 * ((pm >> 1) + 4 * (idx >> 3)) + 2 * (wid >> 1) + r) * (F_ * 2) + xcd * 896 + seg * 128);
  };
  const int s0 = (bid >> 4) * 128 + (wid >> 1) * 32 + ((lane >> 5) | (((lane >> 3) & 3) << 1)) * 4, d0 = (bid & 15) * 64 + (wid & 1) * 32 + (lane & 7) * 4;
  unsigned kvf, chk = 0;
  u32x4 sg[2], cg[2], h0[2], h1[2], uu[2];
  const int ch0 = bid * 4;
  const int rot = wr == 3 ? 0 : wr;
  f32x4 cs0 = {0.f, 0.f, 0.f, 0.f}, cs1 = cs0, cs2 = cs0;
  auto filler = [&](auto UC, auto KC) {
    constexpr int u = decltype(UC)::value, k = decltype(KC)::value, T = u * 28 + k;
    auto xe = [&](int i) { const unsigned w = xr[i >> 3][(i >> 1) & 3]; return (i & 1) ? bf_hi(w) : bf_lo(w); };
    auto red = [&](auto SC) {
      constexpr int st = decltype(SC)::value;
      if constexpr (st == 0) fa = dpp_add<0xB1>(fa + fb_);
      if constexpr (st == 1) fa = dpp_add<0x4E>(fa);
      if constexpr (st == 2) fa = dpp_add<0x141>(fa);
      if constexpr (st == 3) fa = dpp_add<0x140>(fa);
      if constexpr (st == 4) { auto r = __builtin_amdgcn_permlane16_swap(__float_as_uint(fa), __float_as_uint(fa), false, false); fa = __uint_as_float(r[0]) + __uint_as_float(r[1]); }
      if constexpr (st == 5) { auto r = __builtin_amdgcn_permlane32_swap(__float_as_uint(fa), __float_as_uint(fa), false, false); fa = (__uint_as_float(r[0]) + __uint_as_float(r[1])) * (1.0f / 1024.0f); }
      PIN("+v"(fa));
    };
    if constexpr (T0Q < 3 && u == TW1 && k == 11) touch_line(junk1, gT, touch_off(3 - T0Q, 1 + T0Q));
    constexpr int ts = T - (2 * 28 + 7);
    if constexpr (ts == 0) {
      PIN("+v"(ev), "+v"(xr[0]), "+v"(xr[1]), "+v"(junk0));
      unsigned e = __builtin_amdgcn_readfirstlane(ev) * 0x9E3779B1u + 0x7F4A7C15u;
      e ^= e >> 15; e *= 0x85EBCA6Bu; e ^= e >> 13;
      epoch = e;
    }
    if constexpr (ts >= 1 && ts <= 16) {
      if constexpr (ts & 1) { fa += xe(ts - 1); PIN("+v"(fa)); } else { fb_ += xe(ts - 1); PIN("+v"(fb_)); }
    }
    if constexpr (ts >= 17 && ts <= 22) red(IC<ts - 17>{});
    if constexpr (ts == 23) { st_mean = fa; fa = 0.f; fb_ = 0.f; PIN("+v"(st_mean), "+v"(fa), "+v"(fb_), "+v"(xr[0]), "+v"(xr[1])); }
    if constexpr (ts >= 24 && ts <= 39) {
      const float d = xe(ts - 24) - st_mean;
      if constexpr (ts & 1) { fa = __builtin_fmaf(d, d, fa); PIN("+v"(fa)); } else { fb_ = __builtin_fmaf(d, d, fb_); PIN("+v"(fb_)); }
    }
    if constexpr (ts >= 40 && ts <= 45) red(IC<ts - 40>{});
    if constexpr (ts == 46) { fa = rbf(fa) + 1e-5f; PIN("+v"(fa)); }
    if constexpr (ts == 47) { fa = __builtin_amdgcn_sqrtf(rbf(fa)); PIN("+v"(fa)); }
    if constexpr (u == 4 && k == 10) {
      if (lane == 0) __builtin_amdgcn_raw_buffer_store_b64((u32x2){pk_bf16(st_mean, fa), epoch}, wsr, WS_ST + stat_pos(bid * 8 + wid) * 8, 0, 16);
    }
    if constexpr (u == TW1 + 3 && k == 7) PIN("+v"(junk1));
    if constexpr (u >= KVL && u < KVL + 4 && (k == 10 || k == 17 || k == 23 || k == 26)) {
      const unsigned vo = (unsigned)((s0 * D_ + d0) * 2), vp = s0 ? vo - 3 * D_ * 2 : vo;
      constexpr int i = (u - KVL) * 4 + (k == 10 ? 0 : k == 17 ? 1 : k == 23 ? 2 : 3);
      if constexpr (i == 0) gload8<0>(kr[0], kin, vp);
      if constexpr (i == 1) gload8<2048>(kr[1], kin, vp);
      if constexpr (i == 2) gload8<0>(kr[2], kin + 2 * D_, vp);
      if constexpr (i == 3) gload8<0>(kr[3], kin, vo);
      if constexpr (i == 4) gload8<2048>(kr[4], kin, vo);
      if constexpr (i == 5) gload8<0>(kr[5], kin + 2 * D_, vo);
      if constexpr (i == 6) gload8<2048>(kr[6], kin + 2 * D_, vo);
      if constexpr (i == 8) gload8<0>(vr[0], vin, vo);
      if constexpr (i == 9) gload8<2048>(vr[1], vin, vo);
      if constexpr (i == 10) gload8<0>(vr[2], vin + 2 * D_, vo);
      if constexpr (i == 12) gload8<2048>(vr[3], vin + 2 * D_, vo);
      if constexpr (i == 13) gload16<0>(sk[0], shift_k, (unsigned)(d0 * 8));
      if constexpr (i == 14) gload16<16>(sk[1], shift_k, (unsigned)(d0 * 8));
    }
    constexpr int tk = T - ((KVL + 6) * 28 + 7);
    if constexpr (tk == 0) PIN("+v"(kr[0]), "+v"(kr[1]), "+v"(kr[2]), "+v"(kr[3]), "+v"(kr[4]), "+v"(kr[5]), "+v"(kr[6]),
                               "+v"(vr[0]), "+v"(vr[1]), "+v"(vr[2]), "+v"(vr[3]), "+v"(sk[0]), "+v"(sk[1]));
    if constexpr (tk >= 0 && tk < 3) { if (s0 == 0) kr[tk] = (u32x2){0u, 0u}; PIN("+v"(kr[tk])); }
    if constexpr (tk >= 3 && tk < 3 + 76) {
      constexpr int c = (tk - 3) / 19, j = (tk - 3) % 19, jj = c >> 1;
      auto un = [&](unsigned w) { return (c & 1) ? bf_hi(w) : bf_lo(w); };
      if constexpr (j < 3) { kk[2 * j] = un(kr[2 * j][jj]); kk[2 * j + 1] = un(kr[2 * j + 1][jj]); PIN("+v"(kk[2 * j]), "+v"(kk[2 * j + 1])); }
      if constexpr (j == 3) { kk[6] = un(kr[6][jj]); PIN("+v"(kk[6])); }
      if constexpr (j == 4 || j == 5) {
        const unsigned w = sk[jj][2 * (c & 1) + j - 4];
        tt[2 * (j - 4)] = bf_lo(w); tt[2 * (j - 4) + 1] = bf_hi(w); PIN("+v"(tt[2 * (j - 4)]), "+v"(tt[2 * (j - 4) + 1]));
      }
      if constexpr (j >= 6 && j < 18) {
        constexpr int i = (j - 6) / 3, ph = (j - 6) % 3;
        if constexpr (ph == 0) oo[i] = __builtin_fmaf(kk[i + 1], tt[2], kk[i] * tt[3]);
        if constexpr (ph == 1) oo[i] = __builtin_fmaf(kk[i + 3], tt[0], __builtin_fmaf(kk[i + 2], tt[1], oo[i]));
        if constexpr (ph == 2) oo[i] *= un(vr[i][jj]);
        PIN("+v"(oo[i]));
      }
      if constexpr (j == 18) { P[c] = (u32x2){pk_bf16(oo[3], oo[2]), pk_bf16(oo[1], oo[0])}; PIN("+v"(P[c])); }
    }
    if constexpr (u == KVL + 9 && (k == 10 || k == 17)) {
      constexpr int c = k == 10 ? 0 : 1;
      auto rx = __builtin_amdgcn_permlane32_swap(P[c].x, P[c + 2].x, false, false);
      auto ry = __builtin_amdgcn_permlane32_swap(P[c].y, P[c + 2].y, false, false);
      __builtin_amdgcn_raw_buffer_store_b128((u32x4){rx[1], ry[1], rx[0], ry[0]}, wsr,
                                             WS_KVT + ((d0 + c + 2 * (lane >> 5)) * S_ + (s0 & ~7)) * 2, 0, 16);
    }
    if constexpr (u == KVL + 12 && k == 10) {
      if (tid == 0) __builtin_amdgcn_raw_buffer_store_b32(epoch, wsr, WS_KVF + ((bid & 15) * 16 + (bid >> 4)) * 4, 0, 16);
    }
    if constexpr (u == 16 && k == 23) {
      const f32x4 c = wr == 3 ? cs2 : (fr & 4) ? cs1 : cs0;
      const float val = (fr & 2) ? ((fr & 1) ? c[3] : c[2]) : ((fr & 1) ? c[1] : c[0]);
      const int mat = wr < 3 ? (fr >> 2) : (fq >> 1);
      const int f = wc * 56 + (wr < 3 ? wr * 16 + 4 * fq : 48 + 4 * (fq & 1)) + (fr & 3);
      if (fr < (wr < 3 ? 8 : 4)) __builtin_amdgcn_raw_buffer_store_b64((u32x2){__float_as_uint(val), epoch}, wsr, WS_CS + ((pn * 2 + mat) * 112 + f) * 64 + pm * 8, 0, 16);
    }
    if constexpr (u == 20 && k == 11) touch_line(junk2, w2, (unsigned)(((bid * 8 + wid) * 28 + (lane < 28 ? lane : 27)) * 128));
    if constexpr (u == 23 && k == 7) PIN("+v"(junk2));
    if constexpr (u == 24 || u == 27) {
      const unsigned vo_kvf = WS_KVF + (unsigned)((bid >> 4) * 64 + fr * 4);
      const unsigned vo_sg = WS_ST + (unsigned)((pm * 256 + wr * 64 + fr * 4) * 8);
      const unsigned vo_cg = WS_CS + (unsigned)((pn * 224 + ((tid >> 2) < 112 ? (tid >> 2) : 111)) * 64 + (tid & 3) * 16);
      if constexpr (u == 24 && k == 10) gload16s<0>(sg[0], ws, vo_sg);
      if constexpr (u == 24 && k == 12) gload16s<16>(sg[1], ws, vo_sg);
      if constexpr (u == 24 && k == 17) gload16s<0>(cg[0], ws, vo_cg);
      if constexpr (u == 24 && k == 23) gload16s<0>(cg[1], ws + 112 * 64, vo_cg);
      if constexpr (u == 24 && k == 26) gload4s(kvf, ws, vo_kvf);
      if constexpr (u == 27 && k == 7) { PIN("+v"(kvf), "+v"(sg[0]), "+v"(sg[1]), "+v"(cg[0]), "+v"(cg[1])); chk = kvf ^ epoch; PIN("+v"(chk)); }
      if constexpr (u == 27 && k == 8) { chk |= (sg[0].y ^ epoch) | (sg[0].w ^ epoch); PIN("+v"(chk)); }
      if constexpr (u == 27 && k == 9) { chk |= (sg[1].y ^ epoch) | (sg[1].w ^ epoch); PIN("+v"(chk)); }
      if constexpr (u == 27 && k == 10) { chk |= (cg[0].y ^ epoch) | (cg[0].w ^ epoch); PIN("+v"(chk)); }
      if constexpr (u == 27 && k == 11) { chk |= (cg[1].y ^ epoch) | (cg[1].w ^ epoch); PIN("+v"(chk)); }
      if constexpr (u == 27 && k == 12) {
        for (unsigned n = 0; !__all(chk == 0u) && n < SPIN_CAP; ++n) {
          __builtin_amdgcn_s_sleep(2);
          asm volatile("global_load_dwordx4 %0, %5, %8 sc1\n\tglobal_load_dwordx4 %1, %5, %8 offset:16 sc1\n\t"
                       "global_load_dwordx4 %2, %6, %8 sc1\n\tglobal_load_dwordx4 %3, %6, %9 sc1\n\tglobal_load_dword %4, %7, %8 sc1\n\ts_waitcnt vmcnt(0)"
                       : "=&v"(sg[0]), "=&v"(sg[1]), "=&v"(cg[0]), "=&v"(cg[1]), "=&v"(kvf)
                       : "v"(vo_sg), "v"(vo_cg), "v"(vo_kvf), "s"(ws), "s"(ws + 112 * 64) : "memory");
          chk = (kvf ^ epoch) | (sg[0].y ^ epoch) | (sg[0].w ^ epoch) | (sg[1].y ^ epoch) | (sg[1].w ^ epoch) |
                (cg[0].y ^ epoch) | (cg[0].w ^ epoch) | (cg[1].y ^ epoch) | (cg[1].w ^ epoch);
        }
      }
    }
    if constexpr (u == 26 || u == 27) {
      const int n = tid & 255;
      const unsigned vo = (unsigned)((tid >> 8) * 4096 + n * 16), vp = n > 0 ? vo - 16u : vo;
      const char* hb = (const char*)ssm_k + (size_t)ch0 * 4096;
      const char* ub = ws + WS_KVT + (size_t)ch0 * 4096;
      if constexpr (u == 26 && k == 10) gload16<0>(h1[0], hb, vo);
      if constexpr (u == 26 && k == 17) gload16<0>(h0[0], hb, vp);
      if constexpr (u == 26 && k == 23) gload16<0>(h1[1], hb + 8192, vo);
      if constexpr (u == 26 && k == 26) gload16<0>(h0[1], hb + 8192, vp);
      if constexpr (u == 27 && k == 16) gload16s<0>(uu[0], ub, vo);
      if constexpr (u == 27 && k == 24) gload16s<0>(uu[1], ub + 8192, vo);
    }
    if constexpr (u == 28 && k >= 8 && k < 12) {
      constexpr int i = (k - 8) >> 1;
      if constexpr ((k & 1) == 0) { fa = dpp_add<0xB1>(__uint_as_float(cg[i].x) + __uint_as_float(cg[i].z)); PIN("+v"(fa)); }
      else {
        fa = dpp_add<0x4E>(fa);
        if ((tid & 3) == 0 && tid < 448) *(LAS float*)(lds + L_CS + (i * 112 + (tid >> 2)) * 4) = fa;
      }
    }
  };

  int aoff = wr * 8192 + fr * 128 + ((fq ^ (fr >> 1)) * 16);
  const int brow = G1_BOFF + 896 * (4 * (fq >> 1) + (fq & 1)) + 224 * tq + 16 * (7 * wc);
  int boffP[3];
#pragma unroll
  for (int i = 0; i < 3; ++i) boffP[i] = brow + 8 * tp + 32 * (i + rot >= 3 ? i + rot - 3 : i + rot);
  int boffM = brow + 96 + (tp >> 1) * G1_UPOFF + 8 * (tp & 1);
  f32x4 acc[4][7];
  bf16x8 one = {0x3f80, 0x3f80, 0x3f80, 0x3f80, 0x3f80, 0x3f80, 0x3f80, 0x3f80};
  PIN("+v"(one), "+v"(cs0), "+v"(cs1), "+v"(cs2), "+v"(aoff), "+v"(boffM), "+v"(boffP[0]), "+v"(boffP[1]), "+v"(boffP[2]));
  BAR;
  stageB(1, 0); stageB(1, 1); stageA(1, 0, 0); stageA(1, 0, 1);
  stageB(2, 0); stageB(2, 1); stageA(1, 1, 0); stageA(1, 1, 1);
  stageB(3, 0); stageB(3, 1); stageA(2, 0, 0);
  gload4s(ev, ws, WS_E);
  gload16<0>(xr[0], x, (unsigned)((bid * 8 + wid) * (D_ * 2) + lane * 16));
  gload16<1024>(xr[1], x, (unsigned)((bid * 8 + wid) * (D_ * 2) + lane * 16));
  touch_line(junk0, gT, touch_off(T0Q, 1));
  STAMP_AT(18);
  wait_v<15>();
  STAMP_AT(19);
  WAIT_L0;
  BAR;
  STAMP_AT(1);
  bf16x8 A[4], B[7];
  int ab = aoff;
  auto readA = [&](int u, int m) { A[m] = *(LAS bf16x8*)(lds + ab + ((u >> 1) % 3 == 1 ? G1_ASL : 0) + m * 2048); };
  auto readB = [&](int u, int n) { B[n] = tr_frag(lds + (u & 3) * G1_BSL + (n == 6 ? boffM : boffP[n >> 1] + (n & 1) * G1_UPOFF), 2 * 896); };
  readA(0, 0);
#pragma unroll
  for (int n = 0; n < 7; ++n) readB(0, n);
  readA(0, 1); readA(0, 2); readA(0, 3);
  SCHED;
  static_for<0, 32>([&](auto UC) {
    constexpr int u = decltype(UC)::value;
    static_for<0, 28>([&](auto KC) {
      constexpr int k = decltype(KC)::value;
      constexpr int n = k < 7 ? k : k < 14 ? 13 - k : (k - 14) >> 1;
      constexpr int m = k < 7 ? 0 : k < 14 ? 1 : 2 + ((k & 1) ^ (n & 1));
      mfma1<u == 0>(acc[m][n], B[n], A[m]);
      if constexpr (k == 2 && u <= 27) stageA((u + 4) >> 1, u & 1, 1);
      if constexpr (k == 6) {
        if constexpr (u <= 30) wait_v<g1_dma(u - 2) + g1_extra(u - 2) + g1_dma(u - 1) + g1_extra(u - 1)>();
        WAIT_L0;
        BAR;
        if constexpr ((u & 3) == 0 && u > 0 && u <= 24) STAMP_AT((u >> 2) + 1);
        if constexpr (u < 31) {
          ab = aoff ^ (((u + 1) & 1) * 64) ^ (((u + 1) >> 1) % 3 == 2 ? 2 * G1_ASL : 0);
          PIN("+v"(ab));
          readA(u + 1, 0);
        }
      }
      if constexpr (k == 8 && u <= 27) stageB(u + 4, 0);
      if constexpr (k == 13) {
        if constexpr (u < 16)
          asm volatile("s_cmp_lg_u32 %7, %8\n\ts_cbranch_scc1 1f\n\t"
                       "v_mfma_f32_16x16x32_bf16 %0, %3, %6, %0\n\tv_mfma_f32_16x16x32_bf16 %1, %4, %6, %1\n\tv_mfma_f32_16x16x32_bf16 %2, %5, %6, %2\n1:"
                       : "+v"(cs0), "+v"(cs1), "+v"(cs2) : "v"(B[0]), "v"(B[1]), "v"(B[6]), "v"(one), "s"(pm >> 1), "n"(u >> 2) : "scc");
        if constexpr (u < 31) readA(u + 1, 1);
      }
      if constexpr (k == 14 && u <= 26) stageA((u + 5) >> 1, (u + 1) & 1, 0);
      if constexpr (k == 20 && u <= 27) stageB(u + 4, 1);
      if constexpr (k >= 15 && (k & 1) && u < 31) readB(u + 1, (k - 15) >> 1);
      if constexpr (k == 27 && u < 31) { readA(u + 1, 2); readA(u + 1, 3); }
      filler(UC, KC);
      SCHED;
    });
  });
  asm volatile("s_waitcnt vmcnt(0)\n\ts_nop 7\n\ts_nop 7" : "+v"(h0[0]), "+v"(h0[1]), "+v"(h1[0]), "+v"(h1[1]), "+v"(uu[0]), "+v"(uu[1]) :: "memory");
  STAMP_AT(8);

  {
    float mu[4], er[4];
#pragma unroll
    for (int m = 0; m < 4; ++m) { const unsigned w = sg[m >> 1][2 * (m & 1)]; mu[m] = -bf_lo(w); er[m] = -1.44269504089f * __builtin_amdgcn_rcpf(bf_hi(w)); }
    auto act = [&](f32x4 g, f32x4 up, f32x4 cg_, f32x4 cu_, float nm, float e_) {
      g = g + cg_ * nm;
      up = up + cu_ * nm;
      f32x4 e = g * e_;
#pragma unroll
      for (int j = 0; j < 4; ++j) e[j] = __builtin_amdgcn_exp2f(e[j]);
      e = e + 1.0f;
#pragma unroll
      for (int j = 0; j < 4; ++j) e[j] = __builtin_amdgcn_rcpf(e[j]);
      const f32x4 hh = (g * e) * up;
      return (u32x2){pk_bf16(hh[0], hh[1]), pk_bf16(hh[2], hh[3])};
    };
    const int fb = pn * 112 + wc * 56;
    const unsigned hb = WS_H + (unsigned)(pm * 16 + wr * 4) * (F_ / 32 * 1024) + fr * 16;
#pragma unroll
    for (int i = 0; i < 3; ++i) {
      const int ir = i + rot >= 3 ? i + rot - 3 : i + rot;
      const f32x4 cgv = *(LAS f32x4*)(lds + L_CS + (wc * 56 + ir * 16 + 4 * fq) * 4), cuv = *(LAS f32x4*)(lds + L_CS + (112 + wc * 56 + ir * 16 + 4 * fq) * 4);
      const unsigned o = hb + (unsigned)(fq & 1) * (F_ / 32 * 1024) + (unsigned)((fb + ir * 16) / 8 + (fq >> 1)) * 256;
#pragma unroll
      for (int m = 0; m < 4; m += 2) {
        const u32x2 a = act(acc[m][2 * i], acc[m][2 * i + 1], cgv, cuv, mu[m], er[m]);
        const u32x2 b = act(acc[m + 1][2 * i], acc[m + 1][2 * i + 1], cgv, cuv, mu[m + 1], er[m + 1]);
        auto rx = __builtin_amdgcn_permlane16_swap(a.x, b.x, false, false);
        auto ry = __builtin_amdgcn_permlane16_swap(a.y, b.y, false, false);
        __builtin_amdgcn_raw_buffer_store_b128((u32x4){rx[0], ry[0], rx[1], ry[1]}, wsr, o + m * (F_ / 32 * 1024), 0, 16);
      }
    }
    {
      const f32x4 cgv = *(LAS f32x4*)(lds + L_CS + (wc * 56 + 48 + 4 * (fq & 1)) * 4), cuv = *(LAS f32x4*)(lds + L_CS + (112 + wc * 56 + 48 + 4 * (fq & 1)) * 4);
      u32x2 w[2];
#pragma unroll
      for (int m = 0; m < 4; m += 2) {
        f32x4 a = acc[m][6], b = acc[m + 1][6];
#pragma unroll
        for (int j = 0; j < 4; ++j) {
          auto r = __builtin_amdgcn_permlane32_swap(__float_as_uint(a[j]), __float_as_uint(b[j]), false, false);
          a[j] = __uint_as_float(r[0]);
          b[j] = __uint_as_float(r[1]);
        }
        w[m >> 1] = act(a, b, cgv, cuv, (fq >> 1) ? mu[m + 1] : mu[m], (fq >> 1) ? er[m + 1] : er[m]);
      }
      auto rx = __builtin_amdgcn_permlane16_swap(w[0].x, w[1].x, false, false);
      auto ry = __builtin_amdgcn_permlane16_swap(w[0].y, w[1].y, false, false);
      const unsigned o = hb + (unsigned)(2 * (fq & 1) + (fq >> 1)) * (F_ / 32 * 1024) + (unsigned)((fb + 48) / 8) * 256;
      __builtin_amdgcn_raw_buffer_store_b128((u32x4){rx[0], ry[0], rx[1], ry[1]}, wsr, o, 0, 16);
    }
  }
  STAMP_AT(9);

  {
    {
      const int c = tid >> 7, e = (tid >> 4) & 7, p = tid & 15;
      *(LAS u32x4*)(lds + c * CV_CH + e * CV_CS + p * 16) = (u32x4){0u, 0u, 0u, 0u};
      if (tid < 64) *(LAS u32x4*)(lds + (tid >> 4) * CV_CH + 8 * CV_CS + p * 16) = (u32x4){0u, 0u, 0u, 0u};
    }
#pragma unroll
    for (int i = 0; i < 2; ++i) {
      const int c = (tid >> 8) + 2 * i, n = tid & 255;
      if (n == 0) h0[i] = (u32x4){0u, 0u, 0u, 0u};
      unsigned d[8] = {h0[i][0], h0[i][1], h0[i][2], h0[i][3], h1[i][0], h1[i][1], h1[i][2], h1[i][3]};
      unsigned w[7];
#pragma unroll
      for (int j = 0; j < 7; ++j) w[j] = __builtin_amdgcn_alignbit(d[j + 1], d[j], 16);
      LAS unsigned char* base = lds + c * CV_CH + (n + 16) * 16;
#pragma unroll
      for (int e = 0; e < 8; ++e) {
        const int o = e + 1;
        u32x4 t;
        if (o & 1) { const int b = (o - 1) / 2; t = (u32x4){w[b], w[b + 1], w[b + 2], w[b + 3]}; }
        else { const int b = o / 2; t = (u32x4){d[b], d[b + 1], d[b + 2], d[b + 3]}; }
        *(LAS u32x4*)(base + e * CV_CS) = t;
      }
      *(LAS u32x4*)(base + 8 * CV_CS) = uu[i];
    }
  }
  WAIT_L0;
  BAR;
  STAMP_AT(10);
  const int gk = wid >> 2;
  const int pm2 = (xcd >> 1) * 4 + (idx & 3), pn2 = (xcd & 1) * 8 + (idx >> 2);
  unsigned hfl;
  const unsigned vo_hfl = WS_HFL + (unsigned)((pm2 >> 1) * 128 + (lane & 31) * 4);
  f32x4 cacc[8];
  u32x2 ur[4];
  {
    const int c = wid & 3, half = wid >> 2;
    LAS unsigned char* cb = lds + c * CV_CH;
    LAS unsigned char* ap = cb + (fr & 7) * CV_CS + (fr >> 3) * 256 + fq * 16 + half * 256;
    LAS unsigned char* bp = cb + 8 * CV_CS + (32 + fr - fq) * 16 - half * 256 - 192;
#pragma unroll
    for (int t = 0; t < 8; ++t) cacc[t] = (f32x4){0.f, 0.f, 0.f, 0.f};
#pragma unroll
    for (int bb = 0; bb < 4; ++bb) {
      bf16x8 A[8], B[8];
#pragma unroll
      for (int a = 0; a < 8; ++a) {
        A[a] = *(LAS bf16x8*)(ap + bb * 64 + a * 512);
        B[a] = *(LAS bf16x8*)(bp + (3 - bb) * 64 + a * 512);
      }
      if (bb == 1) {
        wait_v<0>();
        if (lane == 0) {
          const unsigned old = __hip_atomic_fetch_add((LAS unsigned*)(lds + L_CNT), 1u, __ATOMIC_RELAXED, __HIP_MEMORY_SCOPE_WORKGROUP);
          if (old == 7u) __builtin_amdgcn_raw_buffer_store_b32(epoch, wsr, WS_HFL + (pm * 32 + pn) * 4, 0, 16);
        }
      }
      if (bb == 3) { SCHED; gload4s(hfl, ws, vo_hfl); SCHED; }
#pragma unroll
      for (int dl = 0; dl < 8; ++dl)
#pragma unroll
        for (int a = 0; a + dl < 8; ++a) cacc[a + dl] = __builtin_amdgcn_mfma_f32_16x16x32_bf16(A[a], B[dl], cacc[a + dl], 0, 0, 0);
    }
#pragma unroll
    for (int cc = 0; cc < 4; ++cc) ur[cc] = *(LAS u32x2*)(lds + cc * CV_CH + 8 * CV_CS + (32 * wid + 16 * (fq >> 1) + fr + 16) * 16 + ((fq & 1) ? 0 : 8));
  }
  asm volatile("s_waitcnt vmcnt(0) lgkmcnt(0)\n\ts_barrier" : "+v"(hfl), "+v"(ur[0]), "+v"(ur[1]), "+v"(ur[2]), "+v"(ur[3]) :: "memory");
  STAMP_AT(11);
  spin_flag(hfl, ws, vo_hfl, epoch);
  STAMP_AT(12);

  const char* gA = ws + WS_H + (size_t)(pm2 * 8 + 2 * wr) * (F_ / 32 * 1024) + gk * 2048;
  const unsigned vA0 = (unsigned)(lane * 16), vA1 = vA0 + (F_ / 32 * 1024);
  unsigned vB[2];
#pragma unroll
  for (int i = 0; i < 2; ++i) {
    const int r = wid * 16 + i * 8 + (lane >> 3);
    const int sw = ((r >> 1) & 1) | (((r >> 3) & 1) << 1);
    vB[i] = (unsigned)(r * (D_ * 2) + pn2 * 128 + ((lane & 7) ^ (sw << 1)) * 16 - i * 1024);
  }
  bf16x8 A2[G2_R][2][2];
  auto stage2 = [&](int u, auto SL) {
    constexpr int sl = decltype(SL)::value;
    const char* ga = gA + (size_t)u * 4096;
    glds2v((const char*)w2 + (size_t)u * (128 * D_ * 2), vB[0], vB[1], ldsb + g2_slot(sl) + wid * 2048);
    gload16s<0>(A2[sl][0][0], ga, vA0);
    gload16s<0>(A2[sl][1][0], ga, vA1);
    gload16s<1024>(A2[sl][0][1], ga, vA0);
    gload16s<1024>(A2[sl][1][1], ga, vA1);
  };
  stage2(0, IC<0>{}); stage2(1, IC<1>{}); stage2(2, IC<2>{}); stage2(3, IC<3>{}); stage2(4, IC<4>{}); stage2(5, IC<5>{});
  {
#pragma unroll
    for (int t = 0; t < 8; ++t) *(LAS f32x4*)(lds + (wid * 8 + t) * 1024 + lane * 16) = cacc[t];
    WAIT_L0;
    BAR;
    const u32x2 dd = *(const u32x2*)(ssm_D + ch0);
    const float Dv[4] = {bf_lo(dd.x), bf_hi(dd.x), bf_lo(dd.y), bf_hi(dd.y)};
    f32x4 y[4];
#pragma unroll
    for (int cc = 0; cc < 4; ++cc) {
      const f32x4 m = *(LAS f32x4*)(lds + (cc * 8 + wid) * 1024 + lane * 16) + *(LAS f32x4*)(lds + ((cc + 4) * 8 + wid) * 1024 + lane * 16);
      const f32x2 ya = rbf2((f32x2){m[0], m[1]}) + rbf2((f32x2){bf_hi(ur[cc].y), bf_lo(ur[cc].y)} * Dv[cc]);
      const f32x2 yb = rbf2((f32x2){m[2], m[3]}) + rbf2((f32x2){bf_hi(ur[cc].x), bf_lo(ur[cc].x)} * Dv[cc]);
      y[cc] = (f32x4){ya.x, ya.y, yb.x, yb.y};
    }
    u32x4 p0, p1;
    p0[0] = pk_bf16(y[0][0], y[1][0]); p0[1] = pk_bf16(y[2][0], y[3][0]); p0[2] = pk_bf16(y[0][1], y[1][1]); p0[3] = pk_bf16(y[2][1], y[3][1]);
    p1[0] = pk_bf16(y[0][2], y[1][2]); p1[1] = pk_bf16(y[2][2], y[3][2]); p1[2] = pk_bf16(y[0][3], y[1][3]); p1[3] = pk_bf16(y[2][3], y[3][3]);
#pragma unroll
    for (int j = 0; j < 4; ++j) {
      auto r = __builtin_amdgcn_permlane32_swap(p0[j], p1[j], false, false);
      p0[j] = r[0]; p1[j] = r[1];
    }
    const unsigned o = WS_Y2 + (unsigned)(bid * 16384 + wid * 2048 + 32 * (2 * fr + (fq & 1)) + 16 * (fq >> 1));
    __builtin_amdgcn_raw_buffer_store_b128(p0, wsr, o, 0, 16);
    __builtin_amdgcn_raw_buffer_store_b128(p1, wsr, o + 1024, 0, 16);
  }
  STAMP_AT(13);

  const int fs = (tq >> 1) | ((fq & 1) << 1);
  int boff[4];
#pragma unroll
  for (int n = 0; n < 4; ++n) boff[n] = (64 * gk + 8 * fq + tq) * 128 + 32 * (n ^ fs) + 8 * tp;
  f32x4 ac2[2][4];
  bf16x8 X[4], Y[4];
#pragma unroll
  for (int n = 0; n < 4; ++n) {
    ac2[0][n] = (f32x4){0.f, 0.f, 0.f, 0.f}; ac2[1][n] = ac2[0][n];
    Y[n] = (bf16x8){0, 0, 0, 0, 0, 0, 0, 0};
  }
  A2[G2_R - 1][0][1] = Y[0]; A2[G2_R - 1][1][1] = Y[0];
  auto mma = [&](const bf16x8& a0, const bf16x8& a1, const bf16x8 (&B)[4]) {
#pragma unroll
    for (int n = 0; n < 4; ++n) ac2[0][n] = __builtin_amdgcn_mfma_f32_16x16x32_bf16(B[n], a0, ac2[0][n], 0, 0, 0);
#pragma unroll
    for (int n = 3; n >= 0; --n) ac2[1][n] = __builtin_amdgcn_mfma_f32_16x16x32_bf16(B[n], a1, ac2[1][n], 0, 0, 0);
  };
  auto body2 = [&](int u, auto SL, auto LOADC, auto VMC) {
    constexpr int sl = decltype(SL)::value, pv = (sl + G2_R - 1) % G2_R;
    constexpr bool LOAD = decltype(LOADC)::value != 0;
    constexpr int VM = decltype(VMC)::value;
    asm volatile("s_waitcnt vmcnt(%4) lgkmcnt(0)\n\ts_barrier" : "+v"(A2[sl][0][0]), "+v"(A2[sl][0][1]), "+v"(A2[sl][1][0]), "+v"(A2[sl][1][1]) : "n"(VM) : "memory");
    LAS unsigned char* sb = lds + g2_slot(sl);
#pragma unroll
    for (int n = 0; n < 4; ++n) X[n] = tr_frag(sb + boff[n], 512);
    SCHED;
    mma(A2[pv][0][1], A2[pv][1][1], Y);
    SCHED;
#pragma unroll
    for (int n = 0; n < 4; ++n) Y[n] = tr_frag(sb + boff[n] + 4096, 512);
    if (LOAD) stage2(u + G2_R - 1, IC<pv>{});
    SCHED;
    mma(A2[sl][0][0], A2[sl][1][0], X);
    SCHED;
  };
  constexpr int VF = (G2_R - 2) * 6;
  unsigned yfl = 0;
  const unsigned vo_yfl = WS_YFL + (unsigned)(pn2 * 64 + fr * 4);
  for (int u = 0; u < 21; u += 7) {
    if (u == 7 && tid == 0) __builtin_amdgcn_raw_buffer_store_b32(epoch, wsr, WS_YFL + bid * 4, 0, 16);
    if (u == 14) asm volatile("global_load_dword %0, %1, %2 sc1" : "+v"(yfl) : "v"(vo_yfl), "s"(ws) : "memory");
    body2(u, IC<0>{}, IC<1>{}, IC<VF>{});
    body2(u + 1, IC<1>{}, IC<1>{}, IC<VF>{});
    body2(u + 2, IC<2>{}, IC<1>{}, IC<VF>{});
    body2(u + 3, IC<3>{}, IC<1>{}, IC<VF>{});
    body2(u + 4, IC<4>{}, IC<1>{}, IC<VF>{});
    body2(u + 5, IC<5>{}, IC<1>{}, IC<VF>{});
    body2(u + 6, IC<6>{}, IC<1>{}, IC<VF>{});
  }
  body2(21, IC<0>{}, IC<1>{}, IC<VF>{});
  asm volatile("" : "+v"(yfl));
  spin_flag(yfl, ws, vo_yfl, epoch);
  const int row = pm2 * 128 + wr * 32 + gk * 16 + fr;
  u32x2 xv[4], yv[4], st;
  {
    const unsigned vx = (unsigned)((row * D_ + pn2 * 64 + 4 * fq) * 2);
    gload8<0>(xv[0], x, vx); gload8<32>(xv[1], x, vx); gload8<64>(xv[2], x, vx); gload8<96>(xv[3], x, vx);
    const unsigned vy = WS_Y2 + (unsigned)(((pn2 * 16 + fq) * S_ + row) * 8);
    gload8s<0>(yv[0], ws, vy); gload8s<0>(yv[1], ws + 65536, vy); gload8s<0>(yv[2], ws + 2 * 65536, vy); gload8s<0>(yv[3], ws + 3 * 65536, vy);
    gload8s<0>(st, ws, WS_ST + (unsigned)(stat_pos(row) * 8));
  }
  body2(22, IC<1>{}, IC<0>{}, IC<30 + 9>{});
  body2(23, IC<2>{}, IC<0>{}, IC<24 + 9>{});
  body2(24, IC<3>{}, IC<0>{}, IC<18 + 9>{});
  body2(25, IC<4>{}, IC<0>{}, IC<12 + 9>{});
  body2(26, IC<5>{}, IC<0>{}, IC<6 + 9>{});
  body2(27, IC<6>{}, IC<0>{}, IC<9>{});
  mma(A2[6][0][1], A2[6][1][1], Y);
  STAMP_AT(14);
  {
    LAS unsigned char* xb = lds + 65536 + wid * 4096 + lane * 16;
#pragma unroll
    for (int n = 0; n < 4; ++n) *(LAS f32x4*)(xb + n * 1024) = gk ? ac2[0][n] : ac2[1][n];
  }
  asm volatile("s_waitcnt vmcnt(0) lgkmcnt(0)\n\ts_barrier" : "+v"(xv[0]), "+v"(xv[1]), "+v"(xv[2]), "+v"(xv[3]),
               "+v"(yv[0]), "+v"(yv[1]), "+v"(yv[2]), "+v"(yv[3]), "+v"(st) :: "memory");
  {
    for (unsigned n = 0; !__all(st.y == epoch) && n < SPIN_CAP; ++n) {
      __builtin_amdgcn_s_sleep(2);
      asm volatile("global_load_dwordx2 %0, %1, %2 sc1\n\ts_waitcnt vmcnt(0)" : "=&v"(st) : "v"(WS_ST + (unsigned)(stat_pos(row) * 8)), "s"(ws) : "memory");
    }
    const float mb = bf_lo(st.x), rs = 1.0f / bf_hi(st.x), rs2 = rs * rs;
    LAS unsigned char* xb = lds + 65536 + (wid ^ 4) * 4096 + lane * 16;
#pragma unroll
    for (int n = 0; n < 4; ++n) {
      const f32x4 b = (*(LAS f32x4*)(xb + n * 1024) + (gk ? ac2[1][n] : ac2[0][n])) * rs2;
      u32x2 w;
#pragma unroll
      for (int j = 0; j < 2; ++j) {
        const f32x2 xf = {bf_lo(xv[n][j]), bf_hi(xv[n][j])};
        const f32x2 xn = rbf2(rbf2(xf - mb) * rs);
        const f32x2 a = rbf2(xn * (f32x2){bf_lo(yv[n][j]), bf_hi(yv[n][j])});
        const f32x2 r = rbf2(xf + a) + rbf2((f32x2){b[2 * j], b[2 * j + 1]});
        w[j] = pk_bf16(r.x, r.y);
      }
      *(u32x2*)(out + (size_t)row * D_ + pn2 * 64 + n * 16 + 4 * fq) = w;
    }
  }
  STAMP_AT(15);
  if (bid == 0 && wid == 0) {
    u32x4 f;
    for (unsigned n = 0; n < SPIN_CAP; ++n) {
      asm volatile("global_load_dwordx4 %0, %1, %2 sc1\n\ts_waitcnt vmcnt(0)" : "=&v"(f) : "v"(WS_KVF + (unsigned)(lane * 16)), "s"(ws) : "memory");
      if (__all(f.x == epoch && f.y == epoch && f.z == epoch && f.w == epoch)) break;
      __builtin_amdgcn_s_sleep(2);
    }
    if (lane == 0) __builtin_amdgcn_raw_buffer_store_b32(ev + 1u, wsr, WS_E, 0, 16);
  }
  STAMP_AT(16);
  STAMP_OUT(20);
}

extern "C" void kernel_launch(void* const* d_in, const int* in_sizes, int n_in, void* d_out, int out_size, void* d_ws, size_t ws_size,
                              hipStream_t stream) {
  k_fused<<<256, 512, 0, stream>>>((const u16*)d_in[0], (const u16*)d_in[1], (const u16*)d_in[2], (const u16*)d_in[3], (const u16*)d_in[4],
                                   (const u16*)d_in[5], (const u16*)d_in[7], (const u16*)d_in[8], (const u16*)d_in[9], (u16*)d_out, (char*)d_ws);
}
```

```cpp
#include <hip/hip_runtime.h>
#include <hip/hip_bf16.h>
#include <type_traits>

typedef unsigned short u16;
typedef short bf16x8 __attribute__((ext_vector_type(8)));
typedef short s16x4 __attribute__((ext_vector_type(4)));
typedef float f32x4 __attribute__((ext_vector_type(4)));
typedef float f32x2 __attribute__((ext_vector_type(2)));
typedef unsigned u32x4 __attribute__((ext_vector_type(4)));
typedef unsigned u32x2 __attribute__((ext_vector_type(2)));
#define LAS __attribute__((address_space(3)))
template <int N> using IC = std::integral_constant<int, N>;

constexpr int S_ = 2048, D_ = 1024, F_ = 3584;

constexpr unsigned WS_E = 0, WS_KVF = 1024, WS_HFL = 2048, WS_YFL = 3072, WS_ST = 4096, WS_XF = 20480, WS_CS = 32768;
constexpr unsigned WS_KVT = 1u << 20, WS_Y2 = 5u << 20, WS_H = 9u << 20, WS_X = 24u << 20, WS_END = 32u << 20;
constexpr unsigned SPIN_CAP = 1u << 18;

__device__ __forceinline__ unsigned pk_bf16(float lo, float hi) {
  typedef __bf16 bf2 __attribute__((ext_vector_type(2)));
  f32x2 v = {lo, hi};
  bf2 r = __builtin_convertvector(v, bf2);
  return __builtin_bit_cast(unsigned, r);
}
__device__ __forceinline__ float bf_lo(unsigned u) { return __uint_as_float(u << 16); }
__device__ __forceinline__ float bf_hi(unsigned u) { return __uint_as_float(u & 0xffff0000u); }
__device__ __forceinline__ float rbf(float x) { return bf_lo(pk_bf16(x, x)); }
__device__ __forceinline__ f32x2 rbf2(f32x2 x) { const unsigned u = pk_bf16(x.x, x.y); return (f32x2){bf_lo(u), bf_hi(u)}; }

#define WAIT_L0 asm volatile("s_waitcnt lgkmcnt(0)" ::: "memory")
#define BAR asm volatile("s_barrier" ::: "memory")
#define SCHED __builtin_amdgcn_sched_barrier(0)
template <int N> __device__ __forceinline__ void wait_v() { asm volatile("s_waitcnt vmcnt(%0)" ::"n"(N) : "memory"); }

#define STAMP_DECL
#define STAMP_AT(i)
#define STAMP_OUT(n)

__device__ __forceinline__ void glds1(const void* g, unsigned vo, unsigned l) {
  asm volatile("s_mov_b32 m0, %2\n\ts_nop 0\n\tglobal_load_lds_dwordx4 %0, %1" ::"v"(vo), "s"(g), "s"(l) : "memory");
}
__device__ __forceinline__ void glds1_56(const void* g, unsigned vo, unsigned l) {
  asm volatile("s_mov_b32 m0, %2\n\ts_mov_b32 exec_hi, 0x00ffffff\n\tglobal_load_lds_dwordx4 %0, %1\n\ts_mov_b32 exec_hi, -1" ::"v"(vo), "s"(g), "s"(l) : "memory");
}
template <int OFF, class T> __device__ __forceinline__ void gload16(T& dst, const void* g, unsigned vo) {
  asm volatile("global_load_dwordx4 %0, %1, %2 offset:%3" : "=v"(dst) : "v"(vo), "s"(g), "n"(OFF) : "memory");
}
template <int OFF, class T> __device__ __forceinline__ void gload16s(T& dst, const void* g, unsigned vo) {
  asm volatile("global_load_dwordx4 %0, %1, %2 offset:%3 sc1" : "=v"(dst) : "v"(vo), "s"(g), "n"(OFF) : "memory");
}
template <int OFF, class T> __device__ __forceinline__ void gload8(T& dst, const void* g, unsigned vo) {
  asm volatile("global_load_dwordx2 %0, %1, %2 offset:%3" : "=v"(dst) : "v"(vo), "s"(g), "n"(OFF) : "memory");
}
template <int OFF, class T> __device__ __forceinline__ void gload8s(T& dst, const void* g, unsigned vo) {
  asm volatile("global_load_dwordx2 %0, %1, %2 offset:%3 sc1" : "=v"(dst) : "v"(vo), "s"(g), "n"(OFF) : "memory");
}
__device__ __forceinline__ void gload4s(unsigned& dst, const void* g, unsigned vo) {
  asm volatile("global_load_dword %0, %1, %2 sc1" : "=v"(dst) : "v"(vo), "s"(g) : "memory");
}
__device__ __forceinline__ void touch_line(unsigned& dst, const void* g, unsigned vo) {
  asm volatile("global_load_dword %0, %1, %2" : "=v"(dst) : "v"(vo), "s"(g) : "memory");
}
__device__ __forceinline__ s16x4 tr_read(LAS unsigned char* p) { return __builtin_amdgcn_ds_read_tr16_b64_v4i16((LAS s16x4*)p); }
__device__ __forceinline__ bf16x8 tr_frag(LAS unsigned char* p, int hi_off) {
  const s16x4 lo = tr_read(p), hi = tr_read(p + hi_off);
  return __builtin_shufflevector(lo, hi, 0, 1, 2, 3, 4, 5, 6, 7);
}
template <int CTRL> __device__ __forceinline__ float dpp_add(float x) {
  return x + __uint_as_float(__builtin_amdgcn_update_dpp(0u, __float_as_uint(x), CTRL, 0xf, 0xf, true));
}
__device__ __forceinline__ float wave_sum(float x) {
  x = dpp_add<0xB1>(x); x = dpp_add<0x4E>(x); x = dpp_add<0x141>(x); x = dpp_add<0x140>(x);
  auto r = __builtin_amdgcn_permlane16_swap(__float_as_uint(x), __float_as_uint(x), false, false);
  x = __uint_as_float(r[0]) + __uint_as_float(r[1]);
  auto t = __builtin_amdgcn_permlane32_swap(__float_as_uint(x), __float_as_uint(x), false, false);
  return __uint_as_float(t[0]) + __uint_as_float(t[1]);
}
__device__ __forceinline__ void spin_flag(unsigned& f, const char* base, unsigned vo, unsigned epoch) {
  for (unsigned n = 0; !__all(f == epoch) && n < SPIN_CAP; ++n) {
    __builtin_amdgcn_s_sleep(2);
    asm volatile("global_load_dword %0, %1, %2 sc1\n\ts_waitcnt vmcnt(0)" : "=&v"(f) : "v"(vo), "s"(base) : "memory");
  }
}

template <bool FIRST> __device__ __forceinline__ void mfma1(f32x4& c, const bf16x8& b, const bf16x8& a) {
  if constexpr (FIRST) asm volatile("v_mfma_f32_16x16x32_bf16 %0, %1, %2, 0" : "=&v"(c) : "v"(b), "v"(a));
  else asm volatile("v_mfma_f32_16x16x32_bf16 %0, %1, %2, %0" : "+v"(c) : "v"(b), "v"(a));
}
#define PIN(...) asm volatile("" : __VA_ARGS__)
template <int I, int N, class F> __device__ __forceinline__ void static_for(F&& f) {
  if constexpr (I < N) { f(IC<I>{}); static_for<I + 1, N>(f); }
}
#define KVL 6
#define T0Q 2
#define TW1 1
#define G2_NOLOAD 0
constexpr int g1_dma(int w) { return (w <= 27 ? 2 : 0) + (w <= 26 ? 2 : 0); }
constexpr int g1_extra(int w) {
  return (w == -1 ? 4 : 0) + (T0Q < 3 && w == TW1 ? 1 : 0) + (w == 4 ? 1 : 0) + (w == KVL ? 4 : w > KVL && w < KVL + 4 ? 3 : 0) + (w == KVL + 9 ? 2 : 0) +
         (w == 16 ? 1 : 0) + (w == 20 ? 1 : 0) + (w == 24 ? 5 : 0) + (w == 26 ? 4 : 0) + (w == 27 ? 2 : 0);
}
__device__ __forceinline__ int stat_pos(int r) { return (r & ~63) | ((r & 15) << 2) | ((r >> 4) & 3); }

constexpr int G1_ASL = 32768, G1_BOFF = 3 * G1_ASL, G1_UPOFF = 7168 + 16, G1_BSL = 2 * 7168 + 16;
constexpr int CV_CS = 4384;
constexpr int CV_CH = 8 * CV_CS + 4352;
constexpr int L_CS = 4 * CV_CH, L_CNT = L_CS + 896;
constexpr int G2_R = 5, G2_NU = F_ / 256;
constexpr int LDS_BYTES = 163840;
static_assert(G1_BOFF + 4 * G1_BSL <= L_CS && L_CNT + 4 <= LDS_BYTES, "lds map");
constexpr int g2_slot(int u) { return (u + 2) % G2_R * 32768; }
constexpr int g2_ops(int w) { return (w < -5 || w > G2_NU - 6 ? 0 : 8) + (w == -3 ? 2 : 0) + (w == 5 ? 1 : 0) + (w == 9 ? 9 : 0); }

__global__ __launch_bounds__(512, 2) void k_fused(const u16* __restrict__ x, const u16* __restrict__ kin, const u16* __restrict__ vin,
                                                  const u16* __restrict__ shift_k, const u16* __restrict__ ssm_k, const u16* __restrict__ ssm_D,
                                                  const u16* __restrict__ w1, const u16* __restrict__ w3, const u16* __restrict__ w2,
                                                  u16* __restrict__ out, char* __restrict__ ws) {
  __shared__ __attribute__((aligned(16))) unsigned char smem[LDS_BYTES];
  STAMP_DECL STAMP_AT(0);
  LAS unsigned char* lds = (LAS unsigned char*)smem;
  const int tid = threadIdx.x, lane = tid & 63, wid = __builtin_amdgcn_readfirstlane(tid >> 6);
  const unsigned ldsb = (unsigned)(size_t)lds;
  const int bid = blockIdx.x, xcd = bid & 7, idx = bid >> 3;
  const int fr = lane & 15, fq = lane >> 4, tq = fr >> 2, tp = lane & 3;
  const __amdgpu_buffer_rsrc_t wsr = __builtin_amdgcn_make_buffer_rsrc(ws, 0, WS_END, 0x00020000);

  const int wr = wid & 3, wc = wid >> 2;
  const int pm = idx & 7, pn = xcd * 4 + (idx >> 3);
  const char* gX = (const char*)x + (size_t)pm * (256 * D_ * 2);
  const unsigned voA0 = (unsigned)((wid * 16 + (lane >> 3)) * (D_ * 2) + (((lane & 7) ^ (lane >> 4)) * 16));
  const unsigned voA1 = (unsigned)((wid * 16 + 8 + (lane >> 3)) * (D_ * 2) + (((lane & 7) ^ (4 + (lane >> 4))) * 16));
  const int Q = (wid & 4) | ((wid & 1) << 1) | ((wid >> 1) & 1);
  const int lrow = lane / 14, lch = lane - lrow * 14;
  const unsigned voB = (unsigned)((Q * 4 + lrow) * (F_ * 2) + pn * 224 + lch * 16);
  auto stageA = [&](int j, int half, int piece) {
    glds1(gX + half * (128 * D_ * 2) + ((j + 8 * (pm & 1)) & 15) * 128, piece ? voA1 : voA0, ldsb + (j % 3) * G1_ASL + half * 16384 + wid * 2048 + piece * 1024);
  };
  auto stageB = [&](int u, int mat) {
    glds1_56((const char*)(mat ? w3 : w1) + (size_t)((u + 16 * (pm & 1)) & 31) * (32 * F_ * 2), voB, ldsb + G1_BOFF + (u & 3) * G1_BSL + wid * 896 + mat * G1_UPOFF);
  };
  stageA(0, 0, 0); stageA(0, 0, 1); stageB(0, 0); stageB(0, 1); stageA(0, 1, 0); stageA(0, 1, 1);
  if (tid == 0) *(LAS unsigned*)(lds + L_CNT) = 0u;

  unsigned ev, epoch = 0, junk0, junk1 = 0, junk2;
  u32x4 xr[2], sk[2];
  u32x2 kr[7], vr[4], P[4];
  float fa = 0.f, fb_ = 0.f, st_mean = 0.f, kk[7], tt[4], oo[4];
  const char* gT = (const char*)((wid & 1) ? w3 : w1) + (size_t)(pm & 1) * (512 * F_ * 2);
  auto touch_off = [&](int nq, int q0) {
    const int l = lane < 14 * nq ? lane : 14 * nq - 1, q = l / 14, r = (l - q * 14) / 7, seg = l - q * 14 - r * 7;
    return (unsigned)((128 * (q + q0) + 8 * ((pm >> 1) + 4 * (idx >> 3)) + 2 * (wid >> 1) + r) * (F_ * 2) + xcd * 896 + seg * 128);
  };
  const int s0 = (bid >> 4) * 128 + (wid >> 1) * 32 + ((lane >> 5) | (((lane >> 3) & 3) << 1)) * 4, d0 = (bid & 15) * 64 + (wid & 1) * 32 + (lane & 7) * 4;
  unsigned kvf, chk = 0;
  u32x4 sg[2], cg[2], h0[2], h1[2], uu[2];
  const int ch0 = bid * 4;
  const int rot = wr == 3 ? 0 : wr;
  f32x4 cs0 = {0.f, 0.f, 0.f, 0.f}, cs1 = cs0, cs2 = cs0;
  auto filler = [&](auto UC, auto KC) {
    constexpr int u = decltype(UC)::value, k = decltype(KC)::value, T = u * 28 + k;
    auto xe = [&](int i) { const unsigned w = xr[i >> 3][(i >> 1) & 3]; return (i & 1) ? bf_hi(w) : bf_lo(w); };
    auto red = [&](auto SC) {
      constexpr int st = decltype(SC)::value;
      if constexpr (st == 0) fa = dpp_add<0xB1>(fa + fb_);
      if constexpr (st == 1) fa = dpp_add<0x4E>(fa);
      if constexpr (st == 2) fa = dpp_add<0x141>(fa);
      if constexpr (st == 3) fa = dpp_add<0x140>(fa);
      if constexpr (st == 4) { auto r = __builtin_amdgcn_permlane16_swap(__float_as_uint(fa), __float_as_uint(fa), false, false); fa = __uint_as_float(r[0]) + __uint_as_float(r[1]); }
      if constexpr (st == 5) { auto r = __builtin_amdgcn_permlane32_swap(__float_as_uint(fa), __float_as_uint(fa), false, false); fa = (__uint_as_float(r[0]) + __uint_as_float(r[1])) * (1.0f / 1024.0f); }
      PIN("+v"(fa));
    };
    if constexpr (T0Q < 3 && u == TW1 && k == 11) touch_line(junk1, gT, touch_off(3 - T0Q, 1 + T0Q));
    constexpr int ts = T - (2 * 28 + 7);
    if constexpr (ts == 0) {
      PIN("+v"(ev), "+v"(xr[0]), "+v"(xr[1]), "+v"(junk0));
      unsigned e = __builtin_amdgcn_readfirstlane(ev) * 0x9E3779B1u + 0x7F4A7C15u;
      e ^= e >> 15; e *= 0x85EBCA6Bu; e ^= e >> 13;
      epoch = e;
    }
    if constexpr (ts >= 1 && ts <= 16) {
      if constexpr (ts & 1) { fa += xe(ts - 1); PIN("+v"(fa)); } else { fb_ += xe(ts - 1); PIN("+v"(fb_)); }
    }
    if constexpr (ts >= 17 && ts <= 22) red(IC<ts - 17>{});
    if constexpr (ts == 23) { st_mean = fa; fa = 0.f; fb_ = 0.f; PIN("+v"(st_mean), "+v"(fa), "+v"(fb_), "+v"(xr[0]), "+v"(xr[1])); }
    if constexpr (ts >= 24 && ts <= 39) {
      const float d = xe(ts - 24) - st_mean;
      if constexpr (ts & 1) { fa = __builtin_fmaf(d, d, fa); PIN("+v"(fa)); } else { fb_ = __builtin_fmaf(d, d, fb_); PIN("+v"(fb_)); }
    }
    if constexpr (ts >= 40 && ts <= 45) red(IC<ts - 40>{});
    if constexpr (ts == 46) { fa = rbf(fa) + 1e-5f; PIN("+v"(fa)); }
    if constexpr (ts == 47) { fa = __builtin_amdgcn_sqrtf(rbf(fa)); PIN("+v"(fa)); }
    if constexpr (u == 4 && k == 10) {
      if (lane == 0) __builtin_amdgcn_raw_buffer_store_b64((u32x2){pk_bf16(st_mean, fa), epoch}, wsr, WS_ST + stat_pos(bid * 8 + wid) * 8, 0, 16);
    }
    if constexpr (u == TW1 + 3 && k == 7) PIN("+v"(junk1));
    if constexpr (u >= KVL && u < KVL + 4 && (k == 10 || k == 17 || k == 23 || k == 26)) {
      const unsigned vo = (unsigned)((s0 * D_ + d0) * 2), vp = s0 ? vo - 3 * D_ * 2 : vo;
      constexpr int i = (u - KVL) * 4 + (k == 10 ? 0 : k == 17 ? 1 : k == 23 ? 2 : 3);
      if constexpr (i == 0) gload8<0>(kr[0], kin, vp);
      if constexpr (i == 1) gload8<2048>(kr[1], kin, vp);
      if constexpr (i == 2) gload8<0>(kr[2], kin + 2 * D_, vp);
      if constexpr (i == 3) gload8<0>(kr[3], kin, vo);
      if constexpr (i == 4) gload8<2048>(kr[4], kin, vo);
      if constexpr (i == 5) gload8<0>(kr[5], kin + 2 * D_, vo);
      if constexpr (i == 6) gload8<2048>(kr[6], kin + 2 * D_, vo);
      if constexpr (i == 8) gload8<0>(vr[0], vin, vo);
      if constexpr (i == 9) gload8<2048>(vr[1], vin, vo);
      if constexpr (i == 10) gload8<0>(vr[2], vin + 2 * D_, vo);
      if constexpr (i == 12) gload8<2048>(vr[3], vin + 2 * D_, vo);
      if constexpr (i == 13) gload16<0>(sk[0], shift_k, (unsigned)(d0 * 8));
      if constexpr (i == 14) gload16<16>(sk[1], shift_k, (unsigned)(d0 * 8));
    }
    constexpr int tk = T - ((KVL + 6) * 28 + 7);
    if constexpr (tk == 0) PIN("+v"(kr[0]), "+v"(kr[1]), "+v"(kr[2]), "+v"(kr[3]), "+v"(kr[4]), "+v"(kr[5]), "+v"(kr[6]),
                               "+v"(vr[0]), "+v"(vr[1]), "+v"(vr[2]), "+v"(vr[3]), "+v"(sk[0]), "+v"(sk[1]));
    if constexpr (tk >= 0 && tk < 3) { if (s0 == 0) kr[tk] = (u32x2){0u, 0u}; PIN("+v"(kr[tk])); }
    if constexpr (tk >= 3 && tk < 3 + 76) {
      constexpr int c = (tk - 3) / 19, j = (tk - 3) % 19, jj = c >> 1;
      auto un = [&](unsigned w) { return (c & 1) ? bf_hi(w) : bf_lo(w); };
      if constexpr (j < 3) { kk[2 * j] = un(kr[2 * j][jj]); kk[2 * j + 1] = un(kr[2 * j + 1][jj]); PIN("+v"(kk[2 * j]), "+v"(kk[2 * j + 1])); }
      if constexpr (j == 3) { kk[6] = un(kr[6][jj]); PIN("+v"(kk[6])); }
      if constexpr (j == 4 || j == 5) {
        const unsigned w = sk[jj][2 * (c & 1) + j - 4];
        tt[2 * (j - 4)] = bf_lo(w); tt[2 * (j - 4) + 1] = bf_hi(w); PIN("+v"(tt[2 * (j - 4)]), "+v"(tt[2 * (j - 4) + 1]));
      }
      if constexpr (j >= 6 && j < 18) {
        constexpr int i = (j - 6) / 3, ph = (j - 6) % 3;
        if constexpr (ph == 0) oo[i] = __builtin_fmaf(kk[i + 1], tt[2], kk[i] * tt[3]);
        if constexpr (ph == 1) oo[i] = __builtin_fmaf(kk[i + 3], tt[0], __builtin_fmaf(kk[i + 2], tt[1], oo[i]));
        if constexpr (ph == 2) oo[i] *= un(vr[i][jj]);
        PIN("+v"(oo[i]));
      }
      if constexpr (j == 18) { P[c] = (u32x2){pk_bf16(oo[3], oo[2]), pk_bf16(oo[1], oo[0])}; PIN("+v"(P[c])); }
    }
    if constexpr (u == KVL + 9 && (k == 10 || k == 17)) {
      constexpr int c = k == 10 ? 0 : 1;
      auto rx = __builtin_amdgcn_permlane32_swap(P[c].x, P[c + 2].x, false, false);
      auto ry = __builtin_amdgcn_permlane32_swap(P[c].y, P[c + 2].y, false, false);
      __builtin_amdgcn_raw_buffer_store_b128((u32x4){rx[1], ry[1], rx[0], ry[0]}, wsr,
                                             WS_KVT + ((d0 + c + 2 * (lane >> 5)) * S_ + (s0 & ~7)) * 2, 0, 16);
    }
    if constexpr (u == KVL + 12 && k == 10) {
      if (tid == 0) __builtin_amdgcn_raw_buffer_store_b32(epoch, wsr, WS_KVF + ((bid & 15) * 16 + (bid >> 4)) * 4, 0, 16);
    }
    if constexpr (u == 16 && k == 23) {
      const f32x4 c = wr == 3 ? cs2 : (fr & 4) ? cs1 : cs0;
      const float val = (fr & 2) ? ((fr & 1) ? c[3] : c[2]) : ((fr & 1) ? c[1] : c[0]);
      const int mat = wr < 3 ? (fr >> 2) : (fq >> 1);
      const int f = wc * 56 + (wr < 3 ? wr * 16 + 4 * fq : 48 + 4 * (fq & 1)) + (fr & 3);
      if (fr < (wr < 3 ? 8 : 4)) __builtin_amdgcn_raw_buffer_store_b64((u32x2){__float_as_uint(val), epoch}, wsr, WS_CS + ((pn * 2 + mat) * 112 + f) * 64 + pm * 8, 0, 16);
    }
    if constexpr (u == 20 && k == 11) touch_line(junk2, w2, (unsigned)(((bid * 8 + wid) * 28 + (lane < 28 ? lane : 27)) * 128));
    if constexpr (u == 23 && k == 7) PIN("+v"(junk2));
    if constexpr (u == 24 || u == 27) {
      const unsigned vo_kvf = WS_KVF + (unsigned)((bid >> 4) * 64 + fr * 4);
      const unsigned vo_sg = WS_ST + (unsigned)((pm * 256 + wr * 64 + fr * 4) * 8);
      const unsigned vo_cg = WS_CS + (unsigned)((pn * 224 + ((tid >> 2) < 112 ? (tid >> 2) : 111)) * 64 + (tid & 3) * 16);
      if constexpr (u == 24 && k == 10) gload16s<0>(sg[0], ws, vo_sg);
      if constexpr (u == 24 && k == 12) gload16s<16>(sg[1], ws, vo_sg);
      if constexpr (u == 24 && k == 17) gload16s<0>(cg[0], ws, vo_cg);
      if constexpr (u == 24 && k == 23) gload16s<0>(cg[1], ws + 112 * 64, vo_cg);
      if constexpr (u == 24 && k == 26) gload4s(kvf, ws, vo_kvf);
      if constexpr (u == 27 && k == 7) { PIN("+v"(kvf), "+v"(sg[0]), "+v"(sg[1]), "+v"(cg[0]), "+v"(cg[1])); chk = kvf ^ epoch; PIN("+v"(chk)); }
      if constexpr (u == 27 && k == 8) { chk |= (sg[0].y ^ epoch) | (sg[0].w ^ epoch); PIN("+v"(chk)); }
      if constexpr (u == 27 && k == 9) { chk |= (sg[1].y ^ epoch) | (sg[1].w ^ epoch); PIN("+v"(chk)); }
      if constexpr (u == 27 && k == 10) { chk |= (cg[0].y ^ epoch) | (cg[0].w ^ epoch); PIN("+v"(chk)); }
      if constexpr (u == 27 && k == 11) { chk |= (cg[1].y ^ epoch) | (cg[1].w ^ epoch); PIN("+v"(chk)); }
      if constexpr (u == 27 && k == 12) {
        for (unsigned n = 0; !__all(chk == 0u) && n < SPIN_CAP; ++n) {
          __builtin_amdgcn_s_sleep(2);
          asm volatile("global_load_dwordx4 %0, %5, %8 sc1\n\tglobal_load_dwordx4 %1, %5, %8 offset:16 sc1\n\t"
                       "global_load_dwordx4 %2, %6, %8 sc1\n\tglobal_load_dwordx4 %3, %6, %9 sc1\n\tglobal_load_dword %4, %7, %8 sc1\n\ts_waitcnt vmcnt(0)"
                       : "=&v"(sg[0]), "=&v"(sg[1]), "=&v"(cg[0]), "=&v"(cg[1]), "=&v"(kvf)
                       : "v"(vo_sg), "v"(vo_cg), "v"(vo_kvf), "s"(ws), "s"(ws + 112 * 64) : "memory");
          chk = (kvf ^ epoch) | (sg[0].y ^ epoch) | (sg[0].w ^ epoch) | (sg[1].y ^ epoch) | (sg[1].w ^ epoch) |
                (cg[0].y ^ epoch) | (cg[0].w ^ epoch) | (cg[1].y ^ epoch) | (cg[1].w ^ epoch);
        }
      }
    }
    if constexpr (u == 26 || u == 27) {
      const int n = tid & 255;
      const unsigned vo = (unsigned)((tid >> 8) * 4096 + n * 16), vp = n > 0 ? vo - 16u : vo;
      const char* hb = (const char*)ssm_k + (size_t)ch0 * 4096;
      const char* ub = ws + WS_KVT + (size_t)ch0 * 4096;
      if constexpr (u == 26 && k == 10) gload16<0>(h1[0], hb, vo);
      if constexpr (u == 26 && k == 17) gload16<0>(h0[0], hb, vp);
      if constexpr (u == 26 && k == 23) gload16<0>(h1[1], hb + 8192, vo);
      if constexpr (u == 26 && k == 26) gload16<0>(h0[1], hb + 8192, vp);
      if constexpr (u == 27 && k == 16) gload16s<0>(uu[0], ub, vo);
      if constexpr (u == 27 && k == 24) gload16s<0>(uu[1], ub + 8192, vo);
    }
    if constexpr (u == 28 && k >= 8 && k < 12) {
      constexpr int i = (k - 8) >> 1;
      if constexpr ((k & 1) == 0) { fa = dpp_add<0xB1>(__uint_as_float(cg[i].x) + __uint_as_float(cg[i].z)); PIN("+v"(fa)); }
      else {
        fa = dpp_add<0x4E>(fa);
        if ((tid & 3) == 0 && tid < 448) *(LAS float*)(lds + L_CS + (i * 112 + (tid >> 2)) * 4) = fa;
      }
    }
  };

  int aoff = wr * 8192 + fr * 128 + ((fq ^ (fr >> 1)) * 16);
  const int brow = G1_BOFF + 896 * (4 * (fq >> 1) + (fq & 1)) + 224 * tq + 16 * (7 * wc);
  int boffP[3];
#pragma unroll
  for (int i = 0; i < 3; ++i) boffP[i] = brow + 8 * tp + 32 * (i + rot >= 3 ? i + rot - 3 : i + rot);
  int boffM = brow + 96 + (tp >> 1) * G1_UPOFF + 8 * (tp & 1);
  f32x4 acc[4][7];
  bf16x8 one = {0x3f80, 0x3f80, 0x3f80, 0x3f80, 0x3f80, 0x3f80, 0x3f80, 0x3f80};
  PIN("+v"(one), "+v"(cs0), "+v"(cs1), "+v"(cs2), "+v"(aoff), "+v"(boffM), "+v"(boffP[0]), "+v"(boffP[1]), "+v"(boffP[2]));
  BAR;
  stageB(1, 0); stageB(1, 1); stageA(1, 0, 0); stageA(1, 0, 1);
  stageB(2, 0); stageB(2, 1); stageA(1, 1, 0); stageA(1, 1, 1);
  stageB(3, 0); stageB(3, 1); stageA(2, 0, 0);
  gload4s(ev, ws, WS_E);
  gload16<0>(xr[0], x, (unsigned)((bid * 8 + wid) * (D_ * 2) + lane * 16));
  gload16<1024>(xr[1], x, (unsigned)((bid * 8 + wid) * (D_ * 2) + lane * 16));
  touch_line(junk0, gT, touch_off(T0Q, 1));
  wait_v<15>();
  WAIT_L0;
  BAR;
  STAMP_AT(1);
  bf16x8 A[4], B[7];
  int ab = aoff;
  auto readA = [&](int u, int m) { A[m] = *(LAS bf16x8*)(lds + ab + ((u >> 1) % 3 == 1 ? G1_ASL : 0) + m * 2048); };
  auto readB = [&](int u, int n) { B[n] = tr_frag(lds + (u & 3) * G1_BSL + (n == 6 ? boffM : boffP[n >> 1] + (n & 1) * G1_UPOFF), 2 * 896); };
  readA(0, 0);
#pragma unroll
  for (int n = 0; n < 7; ++n) readB(0, n);
  readA(0, 1); readA(0, 2); readA(0, 3);
  SCHED;
  static_for<0, 32>([&](auto UC) {
    constexpr int u = decltype(UC)::value;
    static_for<0, 28>([&](auto KC) {
      constexpr int k = decltype(KC)::value;
      constexpr int n = k < 7 ? k : k < 14 ? 13 - k : (k - 14) >> 1;
      constexpr int m = k < 7 ? 0 : k < 14 ? 1 : 2 + ((k & 1) ^ (n & 1));
      mfma1<u == 0>(acc[m][n], B[n], A[m]);
      if constexpr (k == 2 && u <= 27) stageA((u + 4) >> 1, u & 1, 1);
      if constexpr (k == 6) {
        if constexpr (u <= 30) wait_v<g1_dma(u - 2) + g1_extra(u - 2) + g1_dma(u - 1) + g1_extra(u - 1)>();
        WAIT_L0;
        BAR;
        if constexpr ((u & 3) == 0 && u > 0 && u <= 24) STAMP_AT((u >> 2) + 1);
        if constexpr (u < 31) {
          ab = aoff ^ (((u + 1) & 1) * 64) ^ (((u + 1) >> 1) % 3 == 2 ? 2 * G1_ASL : 0);
          PIN("+v"(ab));
          readA(u + 1, 0);
        }
      }
      if constexpr (k == 8 && u <= 27) stageB(u + 4, 0);
      if constexpr (k == 13) {
        if constexpr (u < 16)
          asm volatile("s_cmp_lg_u32 %7, %8\n\ts_cbranch_scc1 1f\n\t"
                       "v_mfma_f32_16x16x32_bf16 %0, %3, %6, %0\n\tv_mfma_f32_16x16x32_bf16 %1, %4, %6, %1\n\tv_mfma_f32_16x16x32_bf16 %2, %5, %6, %2\n1:"
                       : "+v"(cs0), "+v"(cs1), "+v"(cs2) : "v"(B[0]), "v"(B[1]), "v"(B[6]), "v"(one), "s"(pm >> 1), "n"(u >> 2) : "scc");
        if constexpr (u < 31) readA(u + 1, 1);
      }
      if constexpr (k == 14 && u <= 26) stageA((u + 5) >> 1, (u + 1) & 1, 0);
      if constexpr (k == 20 && u <= 27) stageB(u + 4, 1);
      if constexpr (k >= 15 && (k & 1) && u < 31) readB(u + 1, (k - 15) >> 1);
      if constexpr (k == 27 && u < 31) { readA(u + 1, 2); readA(u + 1, 3); }
      filler(UC, KC);
      SCHED;
    });
  });
  asm volatile("s_waitcnt vmcnt(0)\n\ts_nop 7\n\ts_nop 7" : "+v"(h0[0]), "+v"(h0[1]), "+v"(h1[0]), "+v"(h1[1]), "+v"(uu[0]), "+v"(uu[1]) :: "memory");
  STAMP_AT(8);

  {
    float mu[4], er[4];
#pragma unroll
    for (int m = 0; m < 4; ++m) { const unsigned w = sg[m >> 1][2 * (m & 1)]; mu[m] = -bf_lo(w); er[m] = -1.44269504089f * __builtin_amdgcn_rcpf(bf_hi(w)); }
    auto act = [&](f32x4 g, f32x4 up, f32x4 cg_, f32x4 cu_, float nm, float e_) {
      g = g + cg_ * nm;
      up = up + cu_ * nm;
      f32x4 e = g * e_;
#pragma unroll
      for (int j = 0; j < 4; ++j) e[j] = __builtin_amdgcn_exp2f(e[j]);
      e = e + 1.0f;
#pragma unroll
      for (int j = 0; j < 4; ++j) e[j] = __builtin_amdgcn_rcpf(e[j]);
      const f32x4 hh = (g * e) * up;
      return (u32x2){pk_bf16(hh[0], hh[1]), pk_bf16(hh[2], hh[3])};
    };
    const int fb = pn * 112 + wc * 56;
    const unsigned hb = WS_H + (unsigned)(pm * 16 + wr * 4) * (F_ / 32 * 1024) + fr * 16;
#pragma unroll
    for (int i = 0; i < 3; ++i) {
      const int ir = i + rot >= 3 ? i + rot - 3 : i + rot;
      const f32x4 cgv = *(LAS f32x4*)(lds + L_CS + (wc * 56 + ir * 16 + 4 * fq) * 4), cuv = *(LAS f32x4*)(lds + L_CS + (112 + wc * 56 + ir * 16 + 4 * fq) * 4);
      const unsigned o = hb + (unsigned)(fq & 1) * (F_ / 32 * 1024) + (unsigned)((fb + ir * 16) / 8 + (fq >> 1)) * 256;
#pragma unroll
      for (int m = 0; m < 4; m += 2) {
        const u32x2 a = act(acc[m][2 * i], acc[m][2 * i + 1], cgv, cuv, mu[m], er[m]);
        const u32x2 b = act(acc[m + 1][2 * i], acc[m + 1][2 * i + 1], cgv, cuv, mu[m + 1], er[m + 1]);
        auto rx = __builtin_amdgcn_permlane16_swap(a.x, b.x, false, false);
        auto ry = __builtin_amdgcn_permlane16_swap(a.y, b.y, false, false);
        __builtin_amdgcn_raw_buffer_store_b128((u32x4){rx[0], ry[0], rx[1], ry[1]}, wsr, o + m * (F_ / 32 * 1024), 0, 16);
      }
    }
    {
      const f32x4 cgv = *(LAS f32x4*)(lds + L_CS + (wc * 56 + 48 + 4 * (fq & 1)) * 4), cuv = *(LAS f32x4*)(lds + L_CS + (112 + wc * 56 + 48 + 4 * (fq & 1)) * 4);
      u32x2 w[2];
#pragma unroll
      for (int m = 0; m < 4; m += 2) {
        f32x4 a = acc[m][6], b = acc[m + 1][6];
#pragma unroll
        for (int j = 0; j < 4; ++j) {
          auto r = __builtin_amdgcn_permlane32_swap(__float_as_uint(a[j]), __float_as_uint(b[j]), false, false);
          a[j] = __uint_as_float(r[0]);
          b[j] = __uint_as_float(r[1]);
        }
        w[m >> 1] = act(a, b, cgv, cuv, (fq >> 1) ? mu[m + 1] : mu[m], (fq >> 1) ? er[m + 1] : er[m]);
      }
      auto rx = __builtin_amdgcn_permlane16_swap(w[0].x, w[1].x, false, false);
      auto ry = __builtin_amdgcn_permlane16_swap(w[0].y, w[1].y, false, false);
      const unsigned o = hb + (unsigned)(2 * (fq & 1) + (fq >> 1)) * (F_ / 32 * 1024) + (unsigned)((fb + 48) / 8) * 256;
      __builtin_amdgcn_raw_buffer_store_b128((u32x4){rx[0], ry[0], rx[1], ry[1]}, wsr, o, 0, 16);
    }
  }
  STAMP_AT(9);

  {
    {
      const int c = tid >> 7, e = (tid >> 4) & 7, p = tid & 15;
      *(LAS u32x4*)(lds + c * CV_CH + e * CV_CS + p * 16) = (u32x4){0u, 0u, 0u, 0u};
      if (tid < 64) *(LAS u32x4*)(lds + (tid >> 4) * CV_CH + 8 * CV_CS + p * 16) = (u32x4){0u, 0u, 0u, 0u};
    }
#pragma unroll
    for (int i = 0; i < 2; ++i) {
      const int c = (tid >> 8) + 2 * i, n = tid & 255;
      if (n == 0) h0[i] = (u32x4){0u, 0u, 0u, 0u};
      unsigned d[8] = {h0[i][0], h0[i][1], h0[i][2], h0[i][3], h1[i][0], h1[i][1], h1[i][2], h1[i][3]};
      unsigned w[7];
#pragma unroll
      for (int j = 0; j < 7; ++j) w[j] = __builtin_amdgcn_alignbit(d[j + 1], d[j], 16);
      LAS unsigned char* base = lds + c * CV_CH + (n + 16) * 16;
#pragma unroll
      for (int e = 0; e < 8; ++e) {
        const int o = e + 1;
        u32x4 t;
        if (o & 1) { const int b = (o - 1) / 2; t = (u32x4){w[b], w[b + 1], w[b + 2], w[b + 3]}; }
        else { const int b = o / 2; t = (u32x4){d[b], d[b + 1], d[b + 2], d[b + 3]}; }
        *(LAS u32x4*)(base + e * CV_CS) = t;
      }
      *(LAS u32x4*)(base + 8 * CV_CS) = uu[i];
    }
  }
  WAIT_L0;
  BAR;
  STAMP_AT(10);
  const int gk = wid >> 2, kh = xcd >> 2;
  const int pm2 = (xcd & 3) * 4 + (idx & 3), pn2 = idx >> 2;
  unsigned hfl;
  const unsigned vo_hfl = WS_HFL + (unsigned)(((pm2 >> 1) * 32 + kh * 16 + fr) * 4);
  f32x4 cacc[8];
  u32x2 ur[4];
  {
    const int c = wid & 3, half = wid >> 2;
    LAS unsigned char* cb = lds + c * CV_CH;
    LAS unsigned char* ap = cb + (fr & 7) * CV_CS + (fr >> 3) * 256 + fq * 16 + half * 256;
    LAS unsigned char* bp = cb + 8 * CV_CS + (32 + fr - fq) * 16 - half * 256 - 192;
#pragma unroll
    for (int t = 0; t < 8; ++t) cacc[t] = (f32x4){0.f, 0.f, 0.f, 0.f};
#pragma unroll
    for (int bb = 0; bb < 4; ++bb) {
      bf16x8 A[8], B[8];
#pragma unroll
      for (int a = 0; a < 8; ++a) {
        A[a] = *(LAS bf16x8*)(ap + bb * 64 + a * 512);
        B[a] = *(LAS bf16x8*)(bp + (3 - bb) * 64 + a * 512);
      }
      if (bb == 1) {
        wait_v<0>();
        if (lane == 0) {
          const unsigned old = __hip_atomic_fetch_add((LAS unsigned*)(lds + L_CNT), 1u, __ATOMIC_RELAXED, __HIP_MEMORY_SCOPE_WORKGROUP);
          if (old == 7u) __builtin_amdgcn_raw_buffer_store_b32(epoch, wsr, WS_HFL + (pm * 32 + pn) * 4, 0, 16);
        }
      }
      if (bb == 3) { SCHED; gload4s(hfl, ws, vo_hfl); SCHED; }
#pragma unroll
      for (int dl = 0; dl < 8; ++dl)
#pragma unroll
        for (int a = 0; a + dl < 8; ++a) cacc[a + dl] = __builtin_amdgcn_mfma_f32_16x16x32_bf16(A[a], B[dl], cacc[a + dl], 0, 0, 0);
    }
#pragma unroll
    for (int cc = 0; cc < 4; ++cc) ur[cc] = *(LAS u32x2*)(lds + cc * CV_CH + 8 * CV_CS + (32 * wid + 16 * (fq >> 1) + fr + 16) * 16 + ((fq & 1) ? 0 : 8));
  }
  asm volatile("s_waitcnt vmcnt(0) lgkmcnt(0)\n\ts_barrier" : "+v"(hfl), "+v"(ur[0]), "+v"(ur[1]), "+v"(ur[2]), "+v"(ur[3]) :: "memory");
  STAMP_AT(11);

  const char* gA = ws + WS_H + (size_t)(pm2 * 8 + 2 * wr) * (F_ / 32 * 1024) + (kh * 56 + gk * 2) * 1024;
  const unsigned vA[2] = {(unsigned)(lane * 16 + gk * (F_ / 32 * 1024)), (unsigned)(lane * 16 + (gk ^ 1) * (F_ / 32 * 1024))};
  const char* gB = (const char*)w2 + (size_t)kh * (F_ / 2 * D_ * 2) + pn2 * 256;
  unsigned vB[2];
#pragma unroll
  for (int i = 0; i < 2; ++i) vB[i] = (unsigned)((wid * 16 + i * 8 + fq) * (D_ * 2) + (fr ^ ((fq | (i << 2)) << 1)) * 16);
  bf16x8 A2[G2_R][2][2];
  auto stage2B = [&](int u, int i) { glds1(gB + (size_t)(u * 128 + (i & 1) * 4) * (D_ * 2), vB[i >> 1], ldsb + g2_slot(u) + wid * 4096 + i * 1024); };
  auto stage2A = [&](auto UC, auto IC_) {
    constexpr int u = decltype(UC)::value, sub = decltype(IC_)::value >> 1, m = decltype(IC_)::value & 1;
    gload16s<sub * 1024>(A2[u % G2_R][sub][m], gA + u * 4096, vA[m]);
  };
  auto stage2 = [&](auto UC, bool b) {
    if (b) { stage2B(UC, 0); stage2B(UC, 1); stage2B(UC, 2); stage2B(UC, 3); }
    stage2A(UC, IC<0>{}); stage2A(UC, IC<1>{}); stage2A(UC, IC<2>{}); stage2A(UC, IC<3>{});
  };
  stage2B(0, 0); stage2B(0, 1); stage2B(0, 2); stage2B(0, 3);
  spin_flag(hfl, ws, vo_hfl, epoch);
  STAMP_AT(12);
  stage2(IC<0>{}, false); stage2(IC<1>{}, true); stage2(IC<2>{}, true);
  {
#pragma unroll
    for (int t = 0; t < 8; ++t) *(LAS f32x4*)(lds + (wid * 8 + t) * 1024 + lane * 16) = cacc[t];
    WAIT_L0;
    BAR;
    const u32x2 dd = *(const u32x2*)(ssm_D + ch0);
    const float Dv[4] = {bf_lo(dd.x), bf_hi(dd.x), bf_lo(dd.y), bf_hi(dd.y)};
    f32x4 y[4];
#pragma unroll
    for (int cc = 0; cc < 4; ++cc) {
      const f32x4 m = *(LAS f32x4*)(lds + (cc * 8 + wid) * 1024 + lane * 16) + *(LAS f32x4*)(lds + ((cc + 4) * 8 + wid) * 1024 + lane * 16);
      const f32x2 ya = rbf2((f32x2){m[0], m[1]}) + rbf2((f32x2){bf_hi(ur[cc].y), bf_lo(ur[cc].y)} * Dv[cc]);
      const f32x2 yb = rbf2((f32x2){m[2], m[3]}) + rbf2((f32x2){bf_hi(ur[cc].x), bf_lo(ur[cc].x)} * Dv[cc]);
      y[cc] = (f32x4){ya.x, ya.y, yb.x, yb.y};
    }
    u32x4 p0, p1;
    p0[0] = pk_bf16(y[0][0], y[1][0]); p0[1] = pk_bf16(y[2][0], y[3][0]); p0[2] = pk_bf16(y[0][1], y[1][1]); p0[3] = pk_bf16(y[2][1], y[3][1]);
    p1[0] = pk_bf16(y[0][2], y[1][2]); p1[1] = pk_bf16(y[2][2], y[3][2]); p1[2] = pk_bf16(y[0][3], y[1][3]); p1[3] = pk_bf16(y[2][3], y[3][3]);
#pragma unroll
    for (int j = 0; j < 4; ++j) {
      auto r = __builtin_amdgcn_permlane32_swap(p0[j], p1[j], false, false);
      p0[j] = r[0]; p1[j] = r[1];
    }
    const unsigned o = WS_Y2 + (unsigned)(bid * 16384 + wid * 2048 + 32 * (2 * fr + (fq & 1)) + 16 * (fq >> 1));
    __builtin_amdgcn_raw_buffer_store_b128(p0, wsr, o, 0, 16);
    __builtin_amdgcn_raw_buffer_store_b128(p1, wsr, o + 1024, 0, 16);
  }
  STAMP_AT(13);

  int bo[8];
#pragma unroll
  for (int n = 0; n < 8; ++n) { bo[n] = 65536 + (64 * gk + 8 * fq + tq) * 256 + 32 * (n ^ (4 * kh) ^ tq ^ (4 * (fq & 1))) + 8 * tp; PIN("+v"(bo[n])); }
  f32x4 ac2[2][8];
  bf16x8 B2[8];
  auto read2 = [&](auto UC, int sub, auto NC) {
    constexpr int u = decltype(UC)::value, n = decltype(NC)::value;
    constexpr int d = (g2_slot(u) & ~65535) - (g2_slot(u > 0 ? u - 1 : 0) & ~65535);
    if (sub == 0 && d != 0) { bo[n] += d; PIN("+v"(bo[n])); }
    B2[n] = tr_frag(lds + bo[n] + (g2_slot(u) & 65535) + sub * 8192, 1024);
  };
  const int pc = pn2 * 2 + kh;
  const int row = pm2 * 128 + wr * 32 + gk * 16 + fr;
  unsigned yfl;
  const unsigned vo_yfl = WS_YFL + (unsigned)(pc * 64 + fr * 4);
  u32x2 xv[4], yv[4], st;
  asm volatile("s_waitcnt vmcnt(18) lgkmcnt(0)\n\ts_barrier" : "+v"(A2[0][0][0]), "+v"(A2[0][0][1]), "+v"(A2[0][1][0]), "+v"(A2[0][1][1]) :: "memory");
  STAMP_AT(20);
  static_for<0, 8>([&](auto NC) { read2(IC<0>{}, 0, NC); });
  stage2(IC<3>{}, true);
  stage2B(4, 0); stage2B(4, 1); stage2B(4, 2); stage2B(4, 3); stage2A(IC<4>{}, IC<0>{}); stage2A(IC<4>{}, IC<1>{});
  SCHED;
  static_for<0, G2_NU>([&](auto UC) {
    constexpr int u = decltype(UC)::value, sl = u % G2_R;
    static_for<0, 32>([&](auto KC) {
      constexpr int k = decltype(KC)::value, sub = k >> 4, n = (k & 15) >> 1, m = (k & 1) ^ (n & 1);
      mfma1<u == 0 && sub == 0>(ac2[m][n], B2[n], A2[sl][sub][m]);
      if constexpr (k < 16 && (k & 1)) read2(UC, 1, IC<n>{});
      if constexpr (!G2_NOLOAD && (k == 2 || k == 6) && u + 4 < G2_NU) stage2A(IC<u + 4>{}, IC<(k == 2 ? 2 : 3)>{});
      if constexpr (k == 19 && u + 1 < G2_NU) {
        constexpr int nx = (u + 1) % G2_R;
        asm volatile("s_waitcnt vmcnt(%4) lgkmcnt(0)\n\ts_barrier" : "+v"(A2[nx][0][0]), "+v"(A2[nx][0][1]), "+v"(A2[nx][1][0]), "+v"(A2[nx][1][1])
                     : "n"(g2_ops(u - 3) + g2_ops(u - 2) + g2_ops(u - 1)) : "memory");
        STAMP_AT(21 + u);
        read2(IC<u + 1>{}, 0, IC<0>{});
      }
      if constexpr (k >= 19 && (k & 1) && u + 1 < G2_NU) read2(IC<u + 1>{}, 0, IC<n>{});
      if constexpr (!G2_NOLOAD && k >= 20 && k <= 26 && !(k & 1) && u + 5 < G2_NU) stage2B(u + 5, (k - 20) >> 1);
      if constexpr (!G2_NOLOAD && (k == 28 || k == 30) && u + 5 < G2_NU) stage2A(IC<u + 5>{}, IC<(k - 28) / 2>{});
      if constexpr (u == 1 && k == 21) {
        if (tid == 0) __builtin_amdgcn_raw_buffer_store_b32(epoch, wsr, WS_YFL + bid * 4, 0, 16);
      }
      if constexpr (u == 5 && k == 21) gload4s(yfl, ws, vo_yfl);
      if constexpr (u == 9 && k == 20) { PIN("+v"(yfl)); spin_flag(yfl, ws, vo_yfl, epoch); }
      if constexpr (u == 9 && k >= 22 && k <= 30) {
        const unsigned vx = (unsigned)((row * D_ + pc * 64 + 4 * fq) * 2), vy = WS_Y2 + (unsigned)(((pc * 16 + fq) * S_ + row) * 8);
        constexpr int i = k - 22;
        if constexpr (i < 4) gload8<32 * i>(xv[i], x, vx);
        else if constexpr (i < 8) gload8s<0>(yv[i - 4], ws + (i - 4) * 65536, vy);
        else gload8s<0>(st, ws, WS_ST + (unsigned)(stat_pos(row) * 8));
      }
      SCHED;
    });
  });
  STAMP_AT(14);
  asm volatile("s_nop 7\n\ts_nop 7" ::: "memory");
  {
    LAS unsigned char* xb = lds + 65536 + wid * 8192 + lane * 16;
#pragma unroll
    for (int n = 7; n >= 0; --n) *(LAS f32x4*)(xb + n * 1024) = ac2[1][n];
  }
  asm volatile("s_waitcnt vmcnt(0) lgkmcnt(0)\n\ts_barrier" : "+v"(xv[0]), "+v"(xv[1]), "+v"(xv[2]), "+v"(xv[3]),
               "+v"(yv[0]), "+v"(yv[1]), "+v"(yv[2]), "+v"(yv[3]), "+v"(st) :: "memory");
  {
    LAS unsigned char* xb = lds + 65536 + (wid ^ 4) * 8192 + lane * 16;
    const unsigned xo = WS_X + (unsigned)(wid * 4096 + lane * 16);
#pragma unroll
    for (int n = 4; n < 8; ++n) {
      const f32x4 b = *(LAS f32x4*)(xb + n * 1024) + ac2[0][n];
      __builtin_amdgcn_raw_buffer_store_b128(__builtin_bit_cast(u32x4, b), wsr, xo + bid * 32768 + (n - 4) * 1024, 0, 16);
    }
    for (unsigned n = 0; !__all(st.y == epoch) && n < SPIN_CAP; ++n) {
      __builtin_amdgcn_s_sleep(2);
      asm volatile("global_load_dwordx2 %0, %1, %2 sc1\n\ts_waitcnt vmcnt(0)" : "=&v"(st) : "v"(WS_ST + (unsigned)(stat_pos(row) * 8)), "s"(ws) : "memory");
    }
    const float mb = bf_lo(st.x), rs = 1.0f / bf_hi(st.x), rs2 = rs * rs;
    f32x4 base[4];
#pragma unroll
    for (int n = 0; n < 4; ++n) {
      ac2[0][n] += *(LAS f32x4*)(xb + n * 1024);
#pragma unroll
      for (int j = 0; j < 2; ++j) {
        const f32x2 xf = {bf_lo(xv[n][j]), bf_hi(xv[n][j])};
        const f32x2 xn = rbf2(rbf2(xf - mb) * rs);
        const f32x2 r = rbf2(xf + rbf2(xn * (f32x2){bf_lo(yv[n][j]), bf_hi(yv[n][j])}));
        base[n][2 * j] = r.x; base[n][2 * j + 1] = r.y;
      }
    }
    PIN("+v"(base[0]), "+v"(base[1]), "+v"(base[2]), "+v"(base[3]), "+v"(ac2[0][0]), "+v"(ac2[0][1]), "+v"(ac2[0][2]), "+v"(ac2[0][3]));
    STAMP_AT(17);
    wait_v<0>();
    if (lane == 0) __builtin_amdgcn_raw_buffer_store_b32(epoch, wsr, WS_XF + (bid * 8 + wid) * 4, 0, 16);
    STAMP_AT(18);
    unsigned xf = ~epoch;
    spin_flag(xf, ws, WS_XF + (unsigned)(((bid ^ 4) * 8 + wid) * 4), epoch);
    STAMP_AT(19);
    f32x4 g[4];
    asm volatile("global_load_dwordx4 %0, %4, %5 sc1\n\tglobal_load_dwordx4 %1, %4, %5 offset:1024 sc1\n\t"
                 "global_load_dwordx4 %2, %4, %5 offset:2048 sc1\n\tglobal_load_dwordx4 %3, %4, %5 offset:3072 sc1\n\ts_waitcnt vmcnt(0)"
                 : "=&v"(g[0]), "=&v"(g[1]), "=&v"(g[2]), "=&v"(g[3]) : "v"(xo), "s"(ws + (size_t)((bid ^ 4) * 32768)) : "memory");
#pragma unroll
    for (int n = 0; n < 4; ++n) {
      const f32x4 o = g[n];
      const f32x4 b = (ac2[0][n] + o) * rs2;
      const f32x2 lo = rbf2((f32x2){b[0], b[1]}), hi = rbf2((f32x2){b[2], b[3]});
      *(u32x2*)(out + (size_t)row * D_ + pc * 64 + n * 16 + 4 * fq) = (u32x2){pk_bf16(base[n][0] + lo.x, base[n][1] + lo.y), pk_bf16(base[n][2] + hi.x, base[n][3] + hi.y)};
    }
  }
  STAMP_AT(15);
  if (bid == 0 && wid == 0) {
    u32x4 f;
    for (unsigned n = 0; n < SPIN_CAP; ++n) {
      asm volatile("global_load_dwordx4 %0, %1, %2 sc1\n\ts_waitcnt vmcnt(0)" : "=&v"(f) : "v"(WS_KVF + (unsigned)(lane * 16)), "s"(ws) : "memory");
      if (__all(f.x == epoch && f.y == epoch && f.z == epoch && f.w == epoch)) break;
      __builtin_amdgcn_s_sleep(2);
    }
    if (lane == 0) __builtin_amdgcn_raw_buffer_store_b32(ev + 1u, wsr, WS_E, 0, 16);
  }
  STAMP_AT(16);
  STAMP_OUT(34);
}

extern "C" void kernel_launch(void* const* d_in, const int* in_sizes, int n_in, void* d_out, int out_size, void* d_ws, size_t ws_size,
                              hipStream_t stream) {
  k_fused<<<256, 512, 0, stream>>>((const u16*)d_in[0], (const u16*)d_in[1], (const u16*)d_in[2], (const u16*)d_in[3], (const u16*)d_in[4],
                                   (const u16*)d_in[5], (const u16*)d_in[7], (const u16*)d_in[8], (const u16*)d_in[9], (u16*)d_out, (char*)d_ws);
}
```

```cpp
#include <hip/hip_runtime.h>
#include <hip/hip_bf16.h>
#include <type_traits>

#define TAILWS 0
#define HAUX 16
#define W2W 30
#define W2N 1
#define NTB 0
#define MAP2 0
typedef unsigned short u16;
typedef short bf16x8 __attribute__((ext_vector_type(8)));
typedef short s16x4 __attribute__((ext_vector_type(4)));
typedef float f32x4 __attribute__((ext_vector_type(4)));
typedef float f32x16 __attribute__((ext_vector_type(16)));
typedef float f32x2 __attribute__((ext_vector_type(2)));
typedef unsigned u32x4 __attribute__((ext_vector_type(4)));
typedef unsigned u32x2 __attribute__((ext_vector_type(2)));
#define LAS __attribute__((address_space(3)))
template <int N> using IC = std::integral_constant<int, N>;

constexpr int S_ = 2048, D_ = 1024, F_ = 3584;

constexpr unsigned WS_E = 0, WS_KVF = 1024, WS_HFL = 2048, WS_YFL = 3072, WS_ST = 4096, WS_XF = 20480, WS_CS = 32768;
constexpr unsigned WS_KVT = 1u << 20, WS_Y2 = 5u << 20, WS_H = 9u << 20, WS_X = 24u << 20, WS_END = 32u << 20;
constexpr unsigned SPIN_CAP = 1u << 18;

__device__ __forceinline__ unsigned pk_bf16(float lo, float hi) {
  typedef __bf16 bf2 __attribute__((ext_vector_type(2)));
  f32x2 v = {lo, hi};
  bf2 r = __builtin_convertvector(v, bf2);
  return __builtin_bit_cast(unsigned, r);
}
__device__ __forceinline__ float bf_lo(unsigned u) { return __uint_as_float(u << 16); }
__device__ __forceinline__ float bf_hi(unsigned u) { return __uint_as_float(u & 0xffff0000u); }
__device__ __forceinline__ float rbf(float x) { return bf_lo(pk_bf16(x, x)); }
__device__ __forceinline__ f32x2 rbf2(f32x2 x) { const unsigned u = pk_bf16(x.x, x.y); return (f32x2){bf_lo(u), bf_hi(u)}; }

#define WAIT_L0 asm volatile("s_waitcnt lgkmcnt(0)" ::: "memory")
#define BAR asm volatile("s_barrier" ::: "memory")
#define SCHED __builtin_amdgcn_sched_barrier(0)
template <int N> __device__ __forceinline__ void wait_v() { asm volatile("s_waitcnt vmcnt(%0)" ::"n"(N) : "memory"); }

#define STAMP_DECL
#define STAMP_AT(i)
#define STAMP_OUT(n)

__device__ __forceinline__ void glds1(const void* g, unsigned vo, unsigned l) {
  asm volatile("s_mov_b32 m0, %2\n\ts_nop 0\n\tglobal_load_lds_dwordx4 %0, %1" ::"v"(vo), "s"(g), "s"(l) : "memory");
}
#define POLI 0
#define POL "nt"
#define NTX 0
__device__ __forceinline__ void glds1n(const void* g, unsigned vo, unsigned l) {
  asm volatile("s_mov_b32 m0, %2\n\ts_nop 0\n\tglobal_load_lds_dwordx4 %0, %1 nt" ::"v"(vo), "s"(g), "s"(l) : "memory");
}
template <bool NT> __device__ __forceinline__ void glds1_56(const void* g, unsigned vo, unsigned l) {
  if constexpr (NT) asm volatile("s_mov_b32 m0, %2\n\ts_mov_b32 exec_hi, 0x00ffffff\n\tglobal_load_lds_dwordx4 %0, %1 " POL "\n\ts_mov_b32 exec_hi, -1" ::"v"(vo), "s"(g), "s"(l) : "memory");
  else asm volatile("s_mov_b32 m0, %2\n\ts_mov_b32 exec_hi, 0x00ffffff\n\tglobal_load_lds_dwordx4 %0, %1\n\ts_mov_b32 exec_hi, -1" ::"v"(vo), "s"(g), "s"(l) : "memory");
}
#define ABL 0
#define XROT 1
#define NTK 0
#define NTW 0
#define NTT 0
#define NTK_S ""
template <int OFF, class T> __device__ __forceinline__ void gload16n(T& dst, const void* g, unsigned vo) {
  asm volatile("global_load_dwordx4 %0, %1, %2 offset:%3" NTK_S : "=v"(dst) : "v"(vo), "s"(g), "n"(OFF) : "memory");
}
template <int OFF, class T> __device__ __forceinline__ void gload8n(T& dst, const void* g, unsigned vo) {
  asm volatile("global_load_dwordx2 %0, %1, %2 offset:%3" NTK_S : "=v"(dst) : "v"(vo), "s"(g), "n"(OFF) : "memory");
}
template <int OFF, class T> __device__ __forceinline__ void gload16(T& dst, const void* g, unsigned vo) {
  asm volatile("global_load_dwordx4 %0, %1, %2 offset:%3" : "=v"(dst) : "v"(vo), "s"(g), "n"(OFF) : "memory");
}
template <int OFF, class T> __device__ __forceinline__ void gload16s(T& dst, const void* g, unsigned vo) {
  asm volatile("global_load_dwordx4 %0, %1, %2 offset:%3 sc1" : "=v"(dst) : "v"(vo), "s"(g), "n"(OFF) : "memory");
}
template <int OFF, class T> __device__ __forceinline__ void gload8(T& dst, const void* g, unsigned vo) {
  asm volatile("global_load_dwordx2 %0, %1, %2 offset:%3" : "=v"(dst) : "v"(vo), "s"(g), "n"(OFF) : "memory");
}
template <int OFF, class T> __device__ __forceinline__ void gload8s(T& dst, const void* g, unsigned vo) {
  asm volatile("global_load_dwordx2 %0, %1, %2 offset:%3 sc1" : "=v"(dst) : "v"(vo), "s"(g), "n"(OFF) : "memory");
}
__device__ __forceinline__ void gload4s(unsigned& dst, const void* g, unsigned vo) {
  asm volatile("global_load_dword %0, %1, %2 sc1" : "=v"(dst) : "v"(vo), "s"(g) : "memory");
}
template <bool NT> __device__ __forceinline__ void touch_line(unsigned& dst, const void* g, unsigned vo) {
  if constexpr (NT) asm volatile("global_load_dword %0, %1, %2 nt" : "=v"(dst) : "v"(vo), "s"(g) : "memory");
  else asm volatile("global_load_dword %0, %1, %2" : "=v"(dst) : "v"(vo), "s"(g) : "memory");
}
__device__ __forceinline__ s16x4 tr_read(LAS unsigned char* p) { return __builtin_amdgcn_ds_read_tr16_b64_v4i16((LAS s16x4*)p); }
__device__ __forceinline__ bf16x8 tr_frag(LAS unsigned char* p, int hi_off) {
  const s16x4 lo = tr_read(p), hi = tr_read(p + hi_off);
  return __builtin_shufflevector(lo, hi, 0, 1, 2, 3, 4, 5, 6, 7);
}
template <int CTRL> __device__ __forceinline__ float dpp_add(float x) {
  return x + __uint_as_float(__builtin_amdgcn_update_dpp(0u, __float_as_uint(x), CTRL, 0xf, 0xf, true));
}
__device__ __forceinline__ float wave_sum(float x) {
  x = dpp_add<0xB1>(x); x = dpp_add<0x4E>(x); x = dpp_add<0x141>(x); x = dpp_add<0x140>(x);
  auto r = __builtin_amdgcn_permlane16_swap(__float_as_uint(x), __float_as_uint(x), false, false);
  x = __uint_as_float(r[0]) + __uint_as_float(r[1]);
  auto t = __builtin_amdgcn_permlane32_swap(__float_as_uint(x), __float_as_uint(x), false, false);
  return __uint_as_float(t[0]) + __uint_as_float(t[1]);
}
__device__ __forceinline__ void spin_flag(unsigned& f, const char* base, unsigned vo, unsigned epoch) {
  for (unsigned n = 0; !__all(f == epoch) && n < SPIN_CAP; ++n) {
    __builtin_amdgcn_s_sleep(2);
    asm volatile("global_load_dword %0, %1, %2 sc1\n\ts_waitcnt vmcnt(0)" : "=&v"(f) : "v"(vo), "s"(base) : "memory");
  }
}

template <bool FIRST> __device__ __forceinline__ void mfma1(f32x4& c, const bf16x8& b, const bf16x8& a) {
  if constexpr (FIRST) asm volatile("v_mfma_f32_16x16x32_bf16 %0, %1, %2, 0" : "=&v"(c) : "v"(b), "v"(a));
  else asm volatile("v_mfma_f32_16x16x32_bf16 %0, %1, %2, %0" : "+v"(c) : "v"(b), "v"(a));
}
template <bool FIRST> __device__ __forceinline__ void mfma2(f32x16& c, const bf16x8& b, const bf16x8& a) {
  if constexpr (FIRST) asm volatile("v_mfma_f32_32x32x16_bf16 %0, %1, %2, 0" : "=&v"(c) : "v"(b), "v"(a));
  else asm volatile("v_mfma_f32_32x32x16_bf16 %0, %1, %2, %0" : "+v"(c) : "v"(b), "v"(a));
}
template <int G> __device__ __forceinline__ f32x4 quad(const f32x16& c) { return __builtin_shufflevector(c, c, 4 * G, 4 * G + 1, 4 * G + 2, 4 * G + 3); }
#define PIN(...) asm volatile("" : __VA_ARGS__)
template <int I, int N, class F> __device__ __forceinline__ void static_for(F&& f) {
  if constexpr (I < N) { f(IC<I>{}); static_for<I + 1, N>(f); }
}
#define KVL 4
#define T0Q 0
#define TW1 (-1)
#define XW 1
#define KVS 17
#define CSW 17
#define HOW 24
#define TQ0 2
#define BLD 3
#define CPUB 20
#define CCHK 94
#define CSTR 3
#define BAS 354
#define XFS 432
#define G2_NOLOAD 0
constexpr int g1_dma(int w) { return (w <= 27 ? 2 : 0) + (w <= 26 ? 2 : 0); }
constexpr int g1_kv(int u, int k) {
  const int w = u - KVL;
  if (w < 0 || w > 7) return -1;
  if (k == 10) return w < 4 ? w : w + 3;
  if (k == 23) return w < 2 ? 11 + w : w >= 4 && w < 7 ? w : -1;
  return -1;
}
constexpr int g1_extra(int w) {
  return (w == -1 && T0Q > 0 ? 1 : 0) + (w == XW ? 3 : 0) + (TW1 >= 0 && w == TW1 ? 1 : 0) + (w == XW + 5 ? 1 : 0) + (g1_kv(w, 10) >= 0) + (g1_kv(w, 23) >= 0) +
         (w == KVS ? 2 : 0) + (w == CSW ? 1 : 0) + (w == HOW ? 5 : 0) + (w == HOW + 3 ? 2 : 0) + (w == 28 ? 4 : 0) + (w >= W2W && w < W2W + W2N ? 1 : 0);
}
__device__ __forceinline__ int stat_pos(int r) { return (r & ~63) | ((r & 15) << 2) | ((r >> 4) & 3); }

constexpr int G1_ASL = 32768, G1_BOFF = 3 * G1_ASL, G1_UPOFF = 7168 + 16, G1_BSL = 2 * 7168 + 16;
constexpr int CV_CS = 4384;
constexpr int CV_CH = 8 * CV_CS + 4352;
constexpr int L_CS = 4 * CV_CH, L_CNT = L_CS + 896;
constexpr int G2_R = 5, G2_NU = F_ / 256;
constexpr int LDS_BYTES = 163840;
static_assert(G1_BOFF + 4 * G1_BSL <= L_CS && L_CNT + 4 <= LDS_BYTES, "lds map");
constexpr int g2_slot(int u) { return (u + 2) % G2_R * 32768; }
constexpr int G2_T0 = 4 * (G2_NU - 4);
constexpr int g2_u(int t) { return t < G2_T0 ? t >> 2 : G2_NU - 4 + (((t - G2_T0) & 7) >> 1); }
constexpr int g2_sub(int t) { return t < G2_T0 ? (t >> 1) & 1 : t & 1; }
constexpr int g2_h(int t) { return t < G2_T0 ? t & 1 : t < G2_T0 + 8; }
#define A4S 5
#define G2_GAP 3
constexpr int G2_YS = 72;
constexpr int g2_nat(int s) {
  const int u = s >> 5, k = s & 31;
  if (s >= 276 && s <= 288 && (s & 3) == 0) return 100 + 4 * 13 + ((s - 276) >> 2);
  if (s < 16) return s & 1 ? (s < 8 ? 200 + 4 * 4 : 100 + 4 * 2) + (s >> 1) : 0;
  if (u <= 8 && k >= 18 && (k & 3) == 2) return 100 + 4 * (u + 4) + ((k - 18) >> 2);
  if (u <= 9 && k < 16 && (k & 3) == 2) return 200 + 4 * (u + 4) + (k >> 2);
  if (s == G2_YS || s == G2_YS + 4) return 2 + (s - G2_YS) / 4;
  if (s == 240) return 1;
  if (s >= 306 && s <= 318 && (s & 3) == 2) return 10 + ((s - 306) >> 2);
  return 0;
}
struct G2Tab { short op[448]; };
constexpr G2Tab g2_make() {
  G2Tab t{};
  int prev = A4S - G2_GAP;
  for (int s = 0; s < 448; ++s) {
    const int o = g2_nat(s);
    if (!o) continue;
    int at = s > prev + G2_GAP ? s : prev + G2_GAP;
    if (o >> 2 == 25 + 3 && at < 17) at = 17;
    t.op[at] = (short)o;
    prev = at;
  }
  return t;
}
constexpr G2Tab G2T = g2_make();
constexpr int g2_op(int s) { return G2T.op[s]; }
constexpr int g2_at(int o) { for (int i = 0; i < 448; ++i) if (g2_op(i) == o) return i; return -1; }
constexpr int g2_after(int u, int s) {
  int c = u < 3 ? 4 * (3 - u) : 0;
  for (int i = 0; i <= s; ++i) { const int o = g2_op(i); c = (o >= 100 && (o % 100) >> 2 == u) ? 0 : c + (o != 0); }
  return c;
}
constexpr int g2_bar(int s) {
  return s < 304 ? ((s & 31) == 16 ? (s >> 5) + 1 : 0) : s == 304 ? 11 : s == 336 ? 12 : s == 352 ? 13 : 0;
}

constexpr bool g2_ok() {
  for (int s = 0; s < 448; ++s)
    if (g2_bar(s) > 2 && (g2_at(100 + 4 * g2_bar(s) + 3) > s - 24 || g2_at(200 + 4 * g2_bar(s) + 3) > s - 24)) return false;
  return g2_at(3) < g2_at(100 + 4 * 6 + 3) && g2_at(1) > 179 + 32 && g2_at(1) < g2_at(100 + 4 * 11 + 3) && g2_at(10) > 305 && g2_at(13) < BAS - 16;
}
static_assert(g2_ok(), "schedule");

__global__ __launch_bounds__(512, 2) void k_fused(const u16* __restrict__ x, const u16* __restrict__ kin, const u16* __restrict__ vin,
                                                  const u16* __restrict__ shift_k, const u16* __restrict__ ssm_k, const u16* __restrict__ ssm_D,
                                                  const u16* __restrict__ w1, const u16* __restrict__ w3, const u16* __restrict__ w2,
                                                  u16* __restrict__ out, char* __restrict__ ws) {
  __shared__ __attribute__((aligned(16))) unsigned char smem[LDS_BYTES];
  STAMP_DECL STAMP_AT(0);
  LAS unsigned char* lds = (LAS unsigned char*)smem;
  const int tid = threadIdx.x, lane = tid & 63, wid = __builtin_amdgcn_readfirstlane(tid >> 6);
  const unsigned ldsb = (unsigned)(size_t)lds;
  const int bid = blockIdx.x, xcd = bid & 7, idx = bid >> 3;
  const int fr = lane & 15, fq = lane >> 4, tq = fr >> 2, tp = lane & 3;
  const __amdgpu_buffer_rsrc_t wsr = __builtin_amdgcn_make_buffer_rsrc(ws, 0, WS_END, 0x00020000);

  const int wr = wid & 3, wc = wid >> 2;
  const int pm = idx & 7, pn = xcd * 4 + (idx >> 3);
  const char* gX = (const char*)x + (size_t)pm * (256 * D_ * 2);
  const unsigned voA0 = (unsigned)((wid * 16 + (lane >> 3)) * (D_ * 2) + (((lane & 7) ^ (lane >> 4)) * 16));
  const unsigned voA1 = (unsigned)((wid * 16 + 8 + (lane >> 3)) * (D_ * 2) + (((lane & 7) ^ (4 + (lane >> 4))) * 16));
  const int Q = (wid & 4) | ((wid & 1) << 1) | ((wid >> 1) & 1);
  const int lrow = lane / 14, lch = lane - lrow * 14;
  const unsigned voB = (unsigned)((Q * 4 + lrow) * (F_ * 2) + pn * 224 + lch * 16);
  const int jrot = 8 * (pm & 1) + XROT * xcd;
  auto stageA = [&](int j, int half, int piece) {
    const char* g = gX + half * (128 * D_ * 2) + ((j + jrot) & 15) * 128;
    const unsigned l = ldsb + (j % 3) * G1_ASL + half * 16384 + wid * 2048 + piece * 1024;
    if (NTX) glds1n(g, piece ? voA1 : voA0, l); else glds1(g, piece ? voA1 : voA0, l);
  };
  auto stageB = [&](int u, int mat) {
    const char* g = (const char*)(mat ? w3 : w1) + (size_t)(unsigned)(((u + 2 * jrot) & 31) * (32 * F_ * 2));
    const unsigned l = ldsb + G1_BOFF + (u & 3) * G1_BSL + wid * 896 + mat * G1_UPOFF;
    if ((u & 15) < NTW) glds1_56<true>(g, voB, l); else glds1_56<false>(g, voB, l);
  };
  stageA(0, 0, 0); stageA(0, 0, 1); stageB(0, 0); stageB(0, 1); stageA(0, 1, 0); stageA(0, 1, 1);
  if (tid == 0) *(LAS unsigned*)(lds + L_CNT) = 0u;

  unsigned ev, epoch = 0, junk0 = 0, junk1 = 0, junk2 = 0;
  u32x4 xr[2], sk[2];
  u32x2 kr[7], vr[4], P[4];
  float fa = 0.f, fb_ = 0.f, st_mean = 0.f, kk[7], tt[4], oo[4];
  const char* gT = (const char*)((wid & 1) ? w3 : w1);
  auto touch_off = [&](int nq, int q0) {
    const int l = lane < 14 * nq ? lane : 14 * nq - 1, q = l / 14, r = (l - q * 14) / 7, seg = l - q * 14 - r * 7;
    return (unsigned)(((128 * (q + q0) + 64 * jrot + 8 * ((pm >> 1) + 4 * (idx >> 3)) + 2 * (wid >> 1) + r) & 1023) * (F_ * 2) + xcd * 896 + seg * 128);
  };
  const int s0 = (bid >> 4) * 128 + (wid >> 1) * 32 + ((lane >> 5) | (((lane >> 3) & 3) << 1)) * 4, d0 = (bid & 15) * 64 + (wid & 1) * 32 + (lane & 7) * 4;
  unsigned kvf, chk = 0;
  u32x4 sg[2], cg[2], h0[2], h1[2], uu[2];
  const int ch0 = bid * 4;
  const int rot = wr == 3 ? 0 : wr;
  f32x4 cs0 = {0.f, 0.f, 0.f, 0.f}, cs1 = cs0, cs2 = cs0;
  f32x2 csv;
  auto filler = [&](auto UC, auto KC) {
    constexpr int u = decltype(UC)::value, k = decltype(KC)::value, T = u * 28 + k;
    auto xe = [&](int i) { const unsigned w = xr[i >> 3][(i >> 1) & 3]; return (i & 1) ? bf_hi(w) : bf_lo(w); };
    auto red = [&](auto SC) {
      constexpr int st = decltype(SC)::value;
      if constexpr (st == 0) fa = dpp_add<0xB1>(fa + fb_);
      if constexpr (st == 1) fa = dpp_add<0x4E>(fa);
      if constexpr (st == 2) fa = dpp_add<0x141>(fa);
      if constexpr (st == 3) fa = dpp_add<0x140>(fa);
      if constexpr (st == 4) { auto r = __builtin_amdgcn_permlane16_swap(__float_as_uint(fa), __float_as_uint(fa), false, false); fa = __uint_as_float(r[0]) + __uint_as_float(r[1]); }
      if constexpr (st == 5) { auto r = __builtin_amdgcn_permlane32_swap(__float_as_uint(fa), __float_as_uint(fa), false, false); fa = (__uint_as_float(r[0]) + __uint_as_float(r[1])) * (1.0f / 1024.0f); }
      PIN("+v"(fa));
    };
    if constexpr (u == TW1 && k == 11) touch_line<false>(junk1, gT, touch_off(4 - TQ0, TQ0));
    if constexpr (u == 2 && k == 7) PIN("+v"(junk0));
    if constexpr (u == XW && k == 9) gload4s(ev, ws, WS_E);
    if constexpr (u == XW && k == 12) gload16<0>(xr[0], x, (unsigned)((bid * 8 + wid) * (D_ * 2) + lane * 16));
    if constexpr (u == XW && k == 17) gload16<1024>(xr[1], x, (unsigned)((bid * 8 + wid) * (D_ * 2) + lane * 16));
    constexpr int ts = T - ((XW + 3) * 28 + 7);
    if constexpr (ts == 0) {
      PIN("+v"(ev), "+v"(xr[0]), "+v"(xr[1]));
      ev = __builtin_amdgcn_readfirstlane(ev);
      PIN("+s"(ev));
      unsigned e = ev * 0x9E3779B1u + 0x7F4A7C15u;
      e ^= e >> 15; e *= 0x85EBCA6Bu; e ^= e >> 13;
      epoch = e;
    }
    if constexpr (ts >= 1 && ts <= 16) {
      if constexpr (ts & 1) { fa += xe(ts - 1); PIN("+v"(fa)); } else { fb_ += xe(ts - 1); PIN("+v"(fb_)); }
    }
    if constexpr (ts >= 17 && ts <= 22) red(IC<ts - 17>{});
    if constexpr (ts == 23) { st_mean = fa; fa = 0.f; fb_ = 0.f; PIN("+v"(st_mean), "+v"(fa), "+v"(fb_), "+v"(xr[0]), "+v"(xr[1])); }
    if constexpr (ts >= 24 && ts <= 39) {
      const float d = xe(ts - 24) - st_mean;
      if constexpr (ts & 1) { fa = __builtin_fmaf(d, d, fa); PIN("+v"(fa)); } else { fb_ = __builtin_fmaf(d, d, fb_); PIN("+v"(fb_)); }
    }
    if constexpr (ts >= 40 && ts <= 45) red(IC<ts - 40>{});
    if constexpr (ts == 46) { fa = rbf(fa) + 1e-5f; PIN("+v"(fa)); }
    if constexpr (ts == 47) { fa = __builtin_amdgcn_sqrtf(rbf(fa)); PIN("+v"(fa)); }
    if constexpr (u == XW + 5 && k == 12) {
      if (lane == 0) __builtin_amdgcn_raw_buffer_store_b64((u32x2){pk_bf16(st_mean, fa), epoch}, wsr, WS_ST + stat_pos(bid * 8 + wid) * 8, 0, 16);
    }
    if constexpr (TW1 >= 0 && u == TW1 + 3 && k == 7) PIN("+v"(junk1));
    if constexpr (g1_kv(u, k) >= 0) {
      const unsigned vo = (unsigned)((s0 * D_ + d0) * 2), vp = s0 ? vo - 3 * D_ * 2 : vo;
      constexpr int i = g1_kv(u, k);
      if constexpr (i == 0) gload8n<0>(kr[0], kin, vp);
      if constexpr (i == 1) gload8n<2048>(kr[1], kin, vp);
      if constexpr (i == 2) gload8n<0>(kr[2], kin + 2 * D_, vp);
      if constexpr (i == 3) gload8n<0>(kr[3], kin, vo);
      if constexpr (i == 4) gload8n<2048>(kr[4], kin, vo);
      if constexpr (i == 5) gload8n<0>(kr[5], kin + 2 * D_, vo);
      if constexpr (i == 6) gload8n<2048>(kr[6], kin + 2 * D_, vo);
      if constexpr (i == 7) gload8n<0>(vr[0], vin, vo);
      if constexpr (i == 8) gload8n<2048>(vr[1], vin, vo);
      if constexpr (i == 9) gload8n<0>(vr[2], vin + 2 * D_, vo);
      if constexpr (i == 10) gload8n<2048>(vr[3], vin + 2 * D_, vo);
      if constexpr (i == 11) gload16<0>(sk[0], shift_k, (unsigned)(d0 * 8));
      if constexpr (i == 12) gload16<16>(sk[1], shift_k, (unsigned)(d0 * 8));
    }
    constexpr int tk = T - ((KVL + 10) * 28 + 7);
    if constexpr (tk == 0) PIN("+v"(kr[0]), "+v"(kr[1]), "+v"(kr[2]), "+v"(kr[3]), "+v"(kr[4]), "+v"(kr[5]), "+v"(kr[6]),
                               "+v"(vr[0]), "+v"(vr[1]), "+v"(vr[2]), "+v"(vr[3]), "+v"(sk[0]), "+v"(sk[1]));
    if constexpr (tk >= 0 && tk < 3) { if (s0 == 0) kr[tk] = (u32x2){0u, 0u}; PIN("+v"(kr[tk])); }
    if constexpr (tk >= 3 && tk < 3 + 76) {
      constexpr int c = (tk - 3) / 19, j = (tk - 3) % 19, jj = c >> 1;
      auto un = [&](unsigned w) { return (c & 1) ? bf_hi(w) : bf_lo(w); };
      if constexpr (j < 3) { kk[2 * j] = un(kr[2 * j][jj]); kk[2 * j + 1] = un(kr[2 * j + 1][jj]); PIN("+v"(kk[2 * j]), "+v"(kk[2 * j + 1])); }
      if constexpr (j == 3) { kk[6] = un(kr[6][jj]); PIN("+v"(kk[6])); }
      if constexpr (j == 4 || j == 5) {
        const unsigned w = sk[jj][2 * (c & 1) + j - 4];
        tt[2 * (j - 4)] = bf_lo(w); tt[2 * (j - 4) + 1] = bf_hi(w); PIN("+v"(tt[2 * (j - 4)]), "+v"(tt[2 * (j - 4) + 1]));
      }
      if constexpr (j >= 6 && j < 18) {
        constexpr int i = (j - 6) / 3, ph = (j - 6) % 3;
        if constexpr (ph == 0) oo[i] = __builtin_fmaf(kk[i + 1], tt[2], kk[i] * tt[3]);
        if constexpr (ph == 1) oo[i] = __builtin_fmaf(kk[i + 3], tt[0], __builtin_fmaf(kk[i + 2], tt[1], oo[i]));
        if constexpr (ph == 2) oo[i] *= un(vr[i][jj]);
        PIN("+v"(oo[i]));
      }
      if constexpr (j == 18) { P[c] = (u32x2){pk_bf16(oo[3], oo[2]), pk_bf16(oo[1], oo[0])}; PIN("+v"(P[c])); }
    }
    if constexpr (u == KVS && (k == 7 || k == 10)) {
      constexpr int c = k == 7 ? 0 : 1;
      auto rx = __builtin_amdgcn_permlane32_swap(P[c].x, P[c + 2].x, false, false);
      auto ry = __builtin_amdgcn_permlane32_swap(P[c].y, P[c + 2].y, false, false);
      if (!(ABL & 2) || lane == 0) __builtin_amdgcn_raw_buffer_store_b128((u32x4){rx[1], ry[1], rx[0], ry[0]}, wsr,
                                             WS_KVT + d0 * 4096 + (s0 >> 5) * 256 + c * 128 + (lane >> 5) * 64 + (s0 & 24) * 2, 0, 16);
    }
    if constexpr (u == KVS + 3 && k == 7) {
      if (tid == 0) __builtin_amdgcn_raw_buffer_store_b32(epoch, wsr, WS_KVF + ((bid & 15) * 16 + (bid >> 4)) * 4, 0, 16);
    }
    if constexpr (u == 16 && k == 7) {
      const f32x4 c = wr == 3 ? cs2 : (fr & 2) ? cs1 : cs0;
      csv = (fr & 1) ? (f32x2){c[2], c[3]} : (f32x2){c[0], c[1]};
      PIN("+v"(csv));
    }
    if constexpr (u == CSW && k == 12) {
      if (fr < (wr < 3 ? 4 : 2))
        __builtin_amdgcn_raw_buffer_store_b128((u32x4){__float_as_uint(csv.x), epoch, __float_as_uint(csv.y), epoch}, wsr,
                                               WS_CS + (pn * 8 + pm) * 1792 + (wc * 7 + wr * 2 + (fr >> 1)) * 128 + fq * 32 + (fr & 1) * 16, 0, 16);
    }
    if constexpr (u >= W2W && u < W2W + W2N && k == 9) {
      constexpr int nl = 28 / (W2N ? W2N : 1);
      asm volatile("global_load_dword %0, %1, %2" : "+v"(junk2) : "v"((unsigned)(((bid * 8 + wid) * nl + (lane < nl ? lane : nl - 1)) * 128)),
                   "s"((const char*)w2 + (size_t)(u - W2W) * (2048 * nl * 128)) : "memory");
    }
    if constexpr (W2W < 28 && u == W2W + W2N + 2 && k == 7) PIN("+v"(junk2));
    if constexpr (u == HOW || u == HOW + 3) {
      const unsigned vo_kvf = WS_KVF + (unsigned)((bid >> 4) * 64 + fr * 4);
      const unsigned vo_sg = WS_ST + (unsigned)((pm * 256 + wr * 64 + fr * 4) * 8);
      const unsigned vo_cg = WS_CS + (unsigned)((pn * 8 + (tid & 7)) * 1792 + ((tid >> 3) < 56 ? (tid >> 3) : 55) * 16);
      if constexpr (u == HOW && k == 10) gload16s<0>(sg[0], ws, vo_sg);
      if constexpr (u == HOW && k == 12) gload16s<16>(sg[1], ws, vo_sg);
      if constexpr (u == HOW && k == 17) gload16s<0>(cg[0], ws, vo_cg);
      if constexpr (u == HOW && k == 23) gload16s<0>(cg[1], ws + 896, vo_cg);
      if constexpr (u == HOW && k == 26) gload4s(kvf, ws, vo_kvf);
      if constexpr (u == HOW + 3 && k == 7) { PIN("+v"(kvf), "+v"(sg[0]), "+v"(sg[1]), "+v"(cg[0]), "+v"(cg[1])); chk = kvf ^ epoch; PIN("+v"(chk)); }
      if constexpr (u == HOW + 3 && k == 8) { chk |= (sg[0].y ^ epoch) | (sg[0].w ^ epoch); PIN("+v"(chk)); }
      if constexpr (u == HOW + 3 && k == 9) { chk |= (sg[1].y ^ epoch) | (sg[1].w ^ epoch); PIN("+v"(chk)); }
      if constexpr (!(ABL & 1) && u == HOW + 3 && k == 10) { chk |= (cg[0].y ^ epoch) | (cg[0].w ^ epoch); PIN("+v"(chk)); }
      if constexpr (!(ABL & 1) && u == HOW + 3 && k == 11) { chk |= (cg[1].y ^ epoch) | (cg[1].w ^ epoch); PIN("+v"(chk)); }
      if constexpr (u == HOW + 3 && k == 12) {
        for (unsigned n = 0; !__all(chk == 0u) && n < SPIN_CAP; ++n) {
          __builtin_amdgcn_s_sleep(2);
          asm volatile("global_load_dwordx4 %0, %5, %8 sc1\n\tglobal_load_dwordx4 %1, %5, %8 offset:16 sc1\n\t"
                       "global_load_dwordx4 %2, %6, %8 sc1\n\tglobal_load_dwordx4 %3, %6, %9 sc1\n\tglobal_load_dword %4, %7, %8 sc1\n\ts_waitcnt vmcnt(0)"
                       : "=&v"(sg[0]), "=&v"(sg[1]), "=&v"(cg[0]), "=&v"(cg[1]), "=&v"(kvf)
                       : "v"(vo_sg), "v"(vo_cg), "v"(vo_kvf), "s"(ws), "s"(ws + 896) : "memory");
          chk = (kvf ^ epoch) | (sg[0].y ^ epoch) | (sg[0].w ^ epoch) | (sg[1].y ^ epoch) | (sg[1].w ^ epoch) |
                (ABL & 1 ? 0u : (cg[0].y ^ epoch) | (cg[0].w ^ epoch) | (cg[1].y ^ epoch) | (cg[1].w ^ epoch));
        }
      }
    }
    if constexpr (u == HOW + 3 || u == 28) {
      const int n = tid & 255;
      const unsigned vo = (unsigned)((tid >> 8) * 4096 + n * 16), vp = n > 0 ? vo - 16u : vo;
      const unsigned vu = (unsigned)((n >> 2) * 256 + (tid >> 8) * 128 + (n & 3) * 16);
      const char* hb = (const char*)ssm_k + (size_t)ch0 * 4096;
      const char* ub = ws + WS_KVT + (size_t)ch0 * 4096;
      if constexpr (u == 28 && k == 12) gload16n<0>(h1[0], hb, vo);
      if constexpr (u == 28 && k == 20) gload16n<0>(h0[0], hb, vp);
      if constexpr (u == 28 && k == 16) gload16n<0>(h1[1], hb + 8192, vo);
      if constexpr (u == 28 && k == 24) gload16n<0>(h0[1], hb + 8192, vp);
      if constexpr (u == HOW + 3 && k == 14) gload16s<0>(uu[0], ub, vu);
      if constexpr (u == HOW + 3 && k == 18) gload16s<64>(uu[1], ub, vu);
    }
    if constexpr (u == HOW + 3 && k >= 20) {
      constexpr int i = (k - 20) >> 2, j = k & 3;
      if constexpr (j == 0) { fa = dpp_add<0xB1>(__uint_as_float(cg[i].x)); fb_ = dpp_add<0xB1>(__uint_as_float(cg[i].z)); }
      if constexpr (j == 1) { fa = dpp_add<0x4E>(fa); fb_ = dpp_add<0x4E>(fb_); }
      if constexpr (j == 2) { fa = dpp_add<0x141>(fa); fb_ = dpp_add<0x141>(fb_); }
      if constexpr (j < 3) PIN("+v"(fa), "+v"(fb_));
      else if ((tid & 7) == 0 && tid < 448) *(LAS f32x2*)(lds + L_CS + i * 448 + tid) = (f32x2){fa, fb_};
    }
  };

  int aoff = wr * 8192 + fr * 128 + ((fq ^ (fr >> 1)) * 16);
  const int brow = G1_BOFF + 896 * (4 * (fq >> 1) + (fq & 1)) + 224 * tq + 16 * (7 * wc);
  int boffP[3];
#pragma unroll
  for (int i = 0; i < 3; ++i) boffP[i] = brow + 8 * tp + 32 * (i + rot >= 3 ? i + rot - 3 : i + rot);
  int boffM = brow + 96 + (tp >> 1) * G1_UPOFF + 8 * (tp & 1);
  f32x4 acc[4][7];
  bf16x8 one = {0x3f80, 0x3f80, 0x3f80, 0x3f80, 0x3f80, 0x3f80, 0x3f80, 0x3f80};
  PIN("+v"(one), "+v"(cs0), "+v"(cs1), "+v"(cs2), "+v"(aoff), "+v"(boffM), "+v"(boffP[0]), "+v"(boffP[1]), "+v"(boffP[2]));
  BAR;
  stageB(1, 0); stageB(1, 1); stageA(1, 0, 0); stageA(1, 0, 1);
  stageB(2, 0); stageB(2, 1); stageA(1, 1, 0); stageA(1, 1, 1);
  stageB(3, 0); stageB(3, 1); stageA(2, 0, 0);
  if constexpr (XW < 0) {
    gload4s(ev, ws, WS_E);
    gload16<0>(xr[0], x, (unsigned)((bid * 8 + wid) * (D_ * 2) + lane * 16));
    gload16<1024>(xr[1], x, (unsigned)((bid * 8 + wid) * (D_ * 2) + lane * 16));
  }
  if constexpr (T0Q > 0) touch_line<NTT>(junk0, gT, touch_off(T0Q, 1));
  wait_v<11 + g1_extra(-1)>();
  WAIT_L0;
  BAR;
  STAMP_AT(1);
  bf16x8 A[4], B[7];
  int ab = aoff;
  auto readA = [&](int u, int m) { A[m] = *(LAS bf16x8*)(lds + ab + ((u >> 1) % 3 == 1 ? G1_ASL : 0) + m * 2048); };
  auto readB = [&](int u, int n) { B[n] = tr_frag(lds + (u & 3) * G1_BSL + (n == 6 ? boffM : boffP[n >> 1] + (n & 1) * G1_UPOFF), 2 * 896); };
  readA(0, 0);
#pragma unroll
  for (int n = 0; n < 7; ++n) readB(0, n);
  readA(0, 1); readA(0, 2); readA(0, 3);
  SCHED;
  static_for<0, 32>([&](auto UC) {
    constexpr int u = decltype(UC)::value;
    static_for<0, 28>([&](auto KC) {
      constexpr int k = decltype(KC)::value;
      constexpr int n = k < 7 ? k : k < 14 ? 13 - k : (k - 14) >> 1;
      constexpr int m = k < 7 ? 0 : k < 14 ? 1 : 2 + ((k & 1) ^ (n & 1));
      mfma1<u == 0>(acc[m][n], B[n], A[m]);
      if constexpr (k == 2 && u <= 27) stageA((u + 4) >> 1, u & 1, 1);
      if constexpr (k == 6) {
        if constexpr (u <= 30) wait_v<g1_dma(u - 2) + g1_extra(u - 2) + g1_dma(u - 1) + g1_extra(u - 1)>();
        WAIT_L0;
        BAR;
        if constexpr ((u & 3) == 0 && u > 0 && u <= 24) STAMP_AT((u >> 2) + 1);
        if constexpr (u < 31) {
          ab = aoff ^ (((u + 1) & 1) * 64) ^ (((u + 1) >> 1) % 3 == 2 ? 2 * G1_ASL : 0);
          PIN("+v"(ab));
          readA(u + 1, 0);
        }
      }
      if constexpr (k == 8 && u <= 27) stageB(u + 4, 0);
      if constexpr (k == 13) {
        if constexpr (u < 16)
          asm volatile("s_cmp_lg_u32 %7, %8\n\ts_cbranch_scc1 1f\n\t"
                       "v_mfma_f32_16x16x32_bf16 %0, %3, %6, %0\n\tv_mfma_f32_16x16x32_bf16 %1, %4, %6, %1\n\tv_mfma_f32_16x16x32_bf16 %2, %5, %6, %2\n1:"
                       : "+v"(cs0), "+v"(cs1), "+v"(cs2) : "v"(B[0]), "v"(B[1]), "v"(B[6]), "v"(one), "s"(pm >> 1), "n"(u >> 2) : "scc");
        if constexpr (u < 31) readA(u + 1, 1);
      }
      if constexpr (k == 14 && u <= 26) stageA((u + 5) >> 1, (u + 1) & 1, 0);
      if constexpr (k == 20 && u <= 27) stageB(u + 4, 1);
      if constexpr (k >= 15 && (k & 1) && u < 31) readB(u + 1, (k - 15) >> 1);
      if constexpr (k == 27 && u < 31) { readA(u + 1, 2); readA(u + 1, 3); }
      filler(UC, KC);
      SCHED;
    });
  });
  asm volatile("s_nop 7\n\ts_nop 7" ::: "memory");
  STAMP_AT(8);

  {
    float mu[4], er[4];
#pragma unroll
    for (int m = 0; m < 4; ++m) { const unsigned w = sg[m >> 1][2 * (m & 1)]; mu[m] = -bf_lo(w); er[m] = -1.44269504089f * __builtin_amdgcn_rcpf(bf_hi(w)); }
    auto act = [&](f32x4 g, f32x4 up, f32x4 cg_, f32x4 cu_, float nm, float e_) {
      g = g + cg_ * nm;
      up = up + cu_ * nm;
      f32x4 e = g * e_;
#pragma unroll
      for (int j = 0; j < 4; ++j) e[j] = __builtin_amdgcn_exp2f(e[j]);
      e = e + 1.0f;
#pragma unroll
      for (int j = 0; j < 4; ++j) e[j] = __builtin_amdgcn_rcpf(e[j]);
      const f32x4 hh = (g * e) * up;
      return (u32x2){pk_bf16(hh[0], hh[1]), pk_bf16(hh[2], hh[3])};
    };
    auto build = [&](int i, int e0, int e1) {
      SCHED;
      const int c = (tid >> 8) + 2 * i, n = tid & 255;
      if (e0 == 0 && n == 0) h0[i] = (u32x4){0u, 0u, 0u, 0u};
      const unsigned d[8] = {h0[i][0], h0[i][1], h0[i][2], h0[i][3], h1[i][0], h1[i][1], h1[i][2], h1[i][3]};
      LAS unsigned char* base = lds + c * CV_CH + (n + 16) * 16;
#pragma unroll
      for (int e = e0; e < e1; ++e) {
        const int b = (e + 1) / 2;
        u32x4 t = uu[i];
        if (e < 8) {
#pragma unroll
          for (int j = 0; j < 4; ++j) t[j] = (e & 1) ? d[b + j] : __builtin_amdgcn_alignbit(d[b + j + 1], d[b + j], 16);
        }
        *(LAS u32x4*)(base + e * CV_CS) = t;
      }
      SCHED;
    };
    {
      const int c = tid >> 7, e = (tid >> 4) & 7, p = tid & 15;
      *(LAS u32x4*)(lds + c * CV_CH + e * CV_CS + p * 16) = (u32x4){0u, 0u, 0u, 0u};
      if (tid < 64) *(LAS u32x4*)(lds + (tid >> 4) * CV_CH + 8 * CV_CS + p * 16) = (u32x4){0u, 0u, 0u, 0u};
    }
    const int fb = pn * 112 + wc * 56;
    const unsigned hb = WS_H + (unsigned)(pm * 16 + wr * 4) * (F_ / 32 * 1024) + fr * 16;
#pragma unroll
    for (int i = 0; i < 3; ++i) {
      const int ir = i + rot >= 3 ? i + rot - 3 : i + rot;
      const f32x4 cgv = *(LAS f32x4*)(lds + L_CS + (wc * 112 + ir * 32 + 4 * fq) * 4), cuv = *(LAS f32x4*)(lds + L_CS + (wc * 112 + ir * 32 + 16 + 4 * fq) * 4);
      const unsigned o = hb + (unsigned)(fq & 1) * (F_ / 32 * 1024) + (unsigned)((fb + ir * 16) / 8 + (fq >> 1)) * 256;
#pragma unroll
      for (int m = 0; m < 4; m += 2) {
        const int q = 2 * i + m / 2 - BLD;
        if (q == 0) STAMP_AT(34);
        if (q == 0) asm volatile("s_waitcnt vmcnt(%6)" : "+v"(h0[0]), "+v"(h0[1]), "+v"(h1[0]), "+v"(h1[1]), "+v"(uu[0]), "+v"(uu[1]) : "n"(BLD + (W2W >= 28 && W2N)) : "memory");
        if (q == 0) STAMP_AT(35);
        if (q >= 0 && q < 3) build(q * 2 / 3, q * 2 % 3 * 3, q * 2 % 3 * 3 + 3);
        const u32x2 a = act(acc[m][2 * i], acc[m][2 * i + 1], cgv, cuv, mu[m], er[m]);
        if (q >= 0 && q < 3) build((q * 2 + 1) / 3, (q * 2 + 1) % 3 * 3, (q * 2 + 1) % 3 * 3 + 3);
        const u32x2 b = act(acc[m + 1][2 * i], acc[m + 1][2 * i + 1], cgv, cuv, mu[m + 1], er[m + 1]);
        auto rx = __builtin_amdgcn_permlane16_swap(a.x, b.x, false, false);
        auto ry = __builtin_amdgcn_permlane16_swap(a.y, b.y, false, false);
        __builtin_amdgcn_raw_buffer_store_b128((u32x4){rx[0], ry[0], rx[1], ry[1]}, wsr, o + m * (F_ / 32 * 1024), 0, HAUX);
        SCHED;
      }
    }
    {
      const f32x4 cgv = *(LAS f32x4*)(lds + L_CS + (wc * 112 + 96 + 4 * (fq & 1)) * 4), cuv = *(LAS f32x4*)(lds + L_CS + (wc * 112 + 104 + 4 * (fq & 1)) * 4);
      u32x2 w[2];
#pragma unroll
      for (int m = 0; m < 4; m += 2) {
        f32x4 a = acc[m][6], b = acc[m + 1][6];
#pragma unroll
        for (int j = 0; j < 4; ++j) {
          auto r = __builtin_amdgcn_permlane32_swap(__float_as_uint(a[j]), __float_as_uint(b[j]), false, false);
          a[j] = __uint_as_float(r[0]);
          b[j] = __uint_as_float(r[1]);
        }
        w[m >> 1] = act(a, b, cgv, cuv, (fq >> 1) ? mu[m + 1] : mu[m], (fq >> 1) ? er[m + 1] : er[m]);
      }
      auto rx = __builtin_amdgcn_permlane16_swap(w[0].x, w[1].x, false, false);
      auto ry = __builtin_amdgcn_permlane16_swap(w[0].y, w[1].y, false, false);
      const unsigned o = hb + (unsigned)(2 * (fq & 1) + (fq >> 1)) * (F_ / 32 * 1024) + (unsigned)((fb + 48) / 8) * 256;
      __builtin_amdgcn_raw_buffer_store_b128((u32x4){rx[0], ry[0], rx[1], ry[1]}, wsr, o, 0, HAUX);
    }
  }
  STAMP_AT(9);
  WAIT_L0;
  BAR;
  STAMP_AT(10);
  const int gk = wid >> 2, kh = xcd >> 2;
  const int pm2 = MAP2 == 0 ? (xcd & 3) * 4 + (idx & 3) : MAP2 == 1 ? (xcd & 1) * 8 + (idx & 7) : idx & 15;
  const int pn2 = MAP2 == 0 ? idx >> 2 : MAP2 == 1 ? (xcd & 2) * 2 + (idx >> 3) : (xcd & 3) * 2 + (idx >> 4);
  unsigned hfl;
  const unsigned vo_hfl = WS_HFL + (unsigned)(((pm2 >> 1) * 32 + kh * 16 + fr) * 4);
  const char* gA = ws + WS_H + (size_t)(pm2 * 8 + 2 * wr) * (F_ / 32 * 1024) + (kh * 56 + gk * 2) * 1024;
  const unsigned vA = (unsigned)((fq & 1) * (F_ / 32 * 1024) + (fq >> 1) * 256 + fr * 16);
  const char* gB = (const char*)w2 + (size_t)kh * (F_ / 2 * D_ * 2) + pn2 * 256;
  const unsigned vB = (unsigned)((wid * 16 + fq) * (D_ * 2) + (fr ^ (fq << 2)) * 16);
  bf16x8 A2[G2_R][4];
  u32x4 Bp[12], x2[2];
  u32x2 st;
  const int pc = pn2 * 2 + kh;
  const int row = pm2 * 128 + wr * 32 + (lane & 31), col = pc * 64 + gk * 32;
  auto stage2B = [&](int u, int i) { glds1(gB + (size_t)(u * 128 + i * 4) * (D_ * 2), vB, ldsb + g2_slot(u) + wid * 4096 + i * 1024); };
  auto stage2A = [&](auto UC, auto IC_) {
    constexpr int u = decltype(UC)::value, i = decltype(IC_)::value;
    gload16s<i * 512>(A2[u % G2_R][i], gA + u * 4096, vA);
  };

  f32x4 cacc[8];
  u32x2 ur[4];
  {
    const int c = wid & 3, half = wid >> 2;
    LAS unsigned char* cb = lds + c * CV_CH;
    LAS unsigned char* ap = cb + (fr & 7) * CV_CS + (fr >> 3) * 256 + fq * 16 + half * 256;
    LAS unsigned char* bp = cb + 8 * CV_CS + (32 + fr - fq) * 16 - half * 256 - 192;
    bf16x8 Ac[8], Bc[8];
    Bc[0] = *(LAS bf16x8*)(bp + 192);
#pragma unroll
    for (int a = 7; a >= 0; --a) Ac[a] = *(LAS bf16x8*)(ap + a * 512);
#pragma unroll
    for (int a = 1; a < 8; ++a) Bc[a] = *(LAS bf16x8*)(bp + 192 + a * 512);
    SCHED;
    static_for<0, 32>([&](auto RC) {
      constexpr int bb = decltype(RC)::value >> 3, dl = decltype(RC)::value & 7;
      static_for<0, 8 - dl>([&](auto JC) {
        constexpr int a = 7 - dl - decltype(JC)::value, sc = bb * 36 + dl * 8 - dl * (dl - 1) / 2 + decltype(JC)::value;
        mfma1<bb == 0 && dl == 0>(cacc[a + dl], Ac[a], Bc[dl]);
        if constexpr (bb < 3 && a == 7 - dl) Ac[a] = *(LAS bf16x8*)(ap + (bb + 1) * 64 + a * 512);
        if constexpr (bb < 3 && a == 0) Bc[dl] = *(LAS bf16x8*)(bp + (2 - bb) * 64 + dl * 512);
        if constexpr (sc == CPUB) {
          STAMP_AT(36);
          wait_v<0>();
          STAMP_AT(37);
          if constexpr (W2W >= 28) PIN("+v"(junk2));
          if (lane == 0) {
            const unsigned old = __hip_atomic_fetch_add((LAS unsigned*)(lds + L_CNT), 1u, __ATOMIC_RELAXED, __HIP_MEMORY_SCOPE_WORKGROUP);
            if (old == 7u) __builtin_amdgcn_raw_buffer_store_b32(epoch, wsr, WS_HFL + (pm * 32 + pn) * 4, 0, 16);
          }
        }
        if constexpr (sc > CPUB && sc <= CPUB + 36 && (sc - CPUB) % 3 == 0) {
          constexpr int i = (sc - CPUB) / 3 - 1;
          gload16<0>(Bp[i], gB + (size_t)(i / 4 * 128 + i % 4 * 4) * (D_ * 2), vB);
        }
        if constexpr (sc == CPUB + 39) {
          const unsigned vx = (unsigned)((row * D_ + col + 8 * (fq >> 1)) * 2);
          gload16<0>(x2[0], x, vx); gload16<32>(x2[1], x, vx);
          gload8s<0>(st, ws, WS_ST + (unsigned)(stat_pos(row) * 8));
        }
        if constexpr (sc == CCHK - 16) gload4s(hfl, ws, vo_hfl);
        if constexpr (sc == CCHK) {
          STAMP_AT(11);
          asm volatile("s_waitcnt vmcnt(0)" : "+v"(hfl), "+v"(Bp[0]), "+v"(Bp[1]), "+v"(Bp[2]), "+v"(Bp[3]), "+v"(Bp[4]), "+v"(Bp[5]), "+v"(Bp[6]),
                       "+v"(Bp[7]), "+v"(Bp[8]), "+v"(Bp[9]), "+v"(Bp[10]), "+v"(Bp[11]), "+v"(x2[0]), "+v"(x2[1]), "+v"(st) :: "memory");
          spin_flag(hfl, ws, vo_hfl, epoch);
          STAMP_AT(12);
        }
        if constexpr (sc > CCHK && sc <= CCHK + 16 * CSTR && (sc - CCHK) % CSTR == 0) {
          constexpr int i = (sc - CCHK) / CSTR - 1;
          stage2A(IC<i / 4>{}, IC<i % 4>{});
        }
        if constexpr (sc >= 136 && sc < 140) ur[sc - 136] = *(LAS u32x2*)(lds + (sc - 136) * CV_CH + 8 * CV_CS + (32 * wid + 16 * (fq >> 1) + fr + 16) * 16 + ((fq & 1) ? 0 : 8));
        SCHED;
      });
    });
  }
  asm volatile("s_waitcnt lgkmcnt(0)\n\ts_barrier" : "+v"(ur[0]), "+v"(ur[1]), "+v"(ur[2]), "+v"(ur[3]) :: "memory");
  auto putB = [&](int i) { *(LAS u32x4*)(lds + g2_slot(i / 4) + wid * 4096 + i % 4 * 1024 + lane * 16) = Bp[i]; };
  putB(0); putB(1); putB(2); putB(3);
  asm volatile("s_nop 7" ::: "memory");
  SCHED;
#pragma unroll
  for (int t = 0; t < 8; ++t) *(LAS f32x4*)(lds + (wid * 8 + t) * 1024 + lane * 16) = cacc[t];
  const u32x2 dd = *(const u32x2*)(ssm_D + ch0);
  const float Dv[4] = {bf_lo(dd.x), bf_hi(dd.x), bf_lo(dd.y), bf_hi(dd.y)};
  float ya, yb;
  unsigned pa, pb;
  u32x4 p0, p1;
  STAMP_AT(13);

  int bo[4];
#pragma unroll
  for (int n = 0; n < 4; ++n) { bo[n] = 65536 + (64 * gk + 8 * (fq >> 1) + tq) * 256 + 64 * (n ^ (2 * kh + gk) ^ tq) + 32 * (fq & 1) + 8 * tp; PIN("+v"(bo[n])); }
  f32x16 ac2[4];
  f32x4 xs[4];
  bf16x8 B2[2][4];
  auto read2 = [&](auto TC, auto NC) {
    constexpr int t = decltype(TC)::value, u = g2_u(t), j = decltype(NC)::value, n = 2 * g2_h(t) + 1 - (j & 1);
    constexpr int d = (g2_slot(u) & ~65535) - (g2_slot(u > 0 ? u - 1 : 0) & ~65535);
    if constexpr (g2_sub(t) == 0 && j < 2 && d != 0) { bo[n] += d; PIN("+v"(bo[n])); }
    B2[t & 1][j] = tr_frag(lds + bo[n] + (g2_slot(u) & 65535) + (2 * g2_sub(t) + (j >> 1)) * 4096, 1024);
  };
  unsigned yfl;
  const unsigned vo_yfl = WS_YFL + (unsigned)(pc * 64 + fr * 4);
  const unsigned xo = WS_X + (unsigned)(wid * 4096 + lane * 16);
  LAS unsigned char* xb = lds + g2_slot(9) + lane * 16;
  u32x2 xv[4], yv[4];
  float mb, rs;
  unsigned xf;
  const unsigned vo_xf = WS_XF + (unsigned)(((bid ^ 4) * 8 + wid) * 4);
  asm volatile("s_waitcnt vmcnt(12) lgkmcnt(0)\n\ts_barrier" : "+v"(A2[0][0]), "+v"(A2[0][1]), "+v"(A2[0][2]), "+v"(A2[0][3]) :: "memory");
  STAMP_AT(20);
  static_for<0, 4>([&](auto NC) { read2(IC<0>{}, NC); });
  static_for<0, 4>([&](auto NC) { read2(IC<1>{}, NC); });
  SCHED;
  static_for<0, 4 * G2_NU>([&](auto TC) {
    constexpr int t = decltype(TC)::value, u = g2_u(t);
    static_for<0, 8>([&](auto JC) {
      constexpr int j = decltype(JC)::value, sb = t * 8 + j, n = j >> 1, op = g2_op(sb), bu = g2_bar(sb);
      if constexpr (!(j & 1)) mfma2<(t < 2 && n < 2)>(ac2[2 * g2_h(t) + 1 - (n & 1)], B2[t & 1][n], A2[u % G2_R][2 * g2_sub(t) + (n >> 1)]);
      if constexpr ((j & 1) && t + 2 < 4 * G2_NU) read2(IC<t + 2>{}, IC<n>{});
      if constexpr (op >= 200) { if constexpr (!G2_NOLOAD) stage2A(IC<(op - 200) / 4>{}, IC<op & 3>{}); }
      else if constexpr (op >= 100) { if constexpr (!G2_NOLOAD) stage2B((op - 100) / 4, op & 3); }
      else if constexpr (op == 1) gload4s(yfl, ws, vo_yfl);
      else if constexpr (op == 2 || op == 3)
        __builtin_amdgcn_raw_buffer_store_b128(op == 2 ? p0 : p1, wsr, WS_Y2 + (unsigned)(bid * 16384 + wid * 2048 + 32 * (2 * fr + (fq & 1)) + 16 * (fq >> 1)) + (op - 2) * 1024, 0, 16);
      else if constexpr (op >= 10) gload8s<0>(yv[op - 10], ws + (op - 10) * 32768, WS_Y2 + (unsigned)(((col / 4 + (fq >> 1)) * S_ + row) * 8));
      if constexpr (bu > 0) {
        constexpr int r0 = bu % G2_R, r1 = (bu == 11 ? 10 : bu) % G2_R;
        if constexpr (bu == 9) WAIT_L0;
        if constexpr (bu == 1) asm volatile("s_waitcnt lgkmcnt(2)" ::: "memory");
        asm volatile("s_waitcnt vmcnt(%4)\n\ts_barrier" : "+v"(A2[r0][0]), "+v"(A2[r0][1]), "+v"(A2[r0][2]), "+v"(A2[r0][3])
                     : "n"(g2_after(bu, sb)) : "memory");
        if constexpr (r0 != r1) PIN("+v"(A2[r1][0]), "+v"(A2[r1][1]), "+v"(A2[r1][2]), "+v"(A2[r1][3]));
        if constexpr (bu != 12) STAMP_AT(20 + (bu > 10 ? (bu + 9) / 2 : bu));
      }
      if constexpr (sb < 8) putB(4 + sb);
      if constexpr (sb >= 6 && sb < 14) cacc[sb - 6] = *(LAS f32x4*)(lds + (((sb - 6) >> 1) + 4 * (sb & 1)) * 8192 + wid * 1024 + lane * 16);
      if constexpr (sb >= 16 && sb < 64) {
        constexpr int cc = (sb - 16) / 12, e = (sb - 16) / 6 % 2 * 2, ph = (sb - 16) % 6;
        const unsigned uw = e ? ur[cc].x : ur[cc].y;
        if constexpr (ph == 0) { ya = cacc[2 * cc][e] + cacc[2 * cc + 1][e]; PIN("+v"(ya)); yb = cacc[2 * cc][e + 1] + cacc[2 * cc + 1][e + 1]; PIN("+v"(yb)); }
        if constexpr (ph == 1) { pa = pk_bf16(ya, yb); ya = bf_hi(uw); yb = bf_lo(uw); PIN("+v"(pa), "+v"(ya), "+v"(yb)); }
        if constexpr (ph == 2) { ya *= Dv[cc]; PIN("+v"(ya)); yb *= Dv[cc]; PIN("+v"(yb)); }
        if constexpr (ph == 3) { pb = pk_bf16(ya, yb); ya = bf_lo(pa); yb = bf_hi(pa); PIN("+v"(pb), "+v"(ya), "+v"(yb)); }
        if constexpr (ph == 4) { cacc[2 * cc][e] = ya + bf_lo(pb); PIN("+v"(cacc[2 * cc])); }
        if constexpr (ph == 5) { cacc[2 * cc][e + 1] = yb + bf_hi(pb); PIN("+v"(cacc[2 * cc])); }
      }
      if constexpr (sb >= 64 && sb < 68) {
        constexpr int j = sb - 64, e = j >> 1, c2 = (j & 1) * 4;
        p0[j] = pk_bf16(cacc[c2][e], cacc[c2 + 2][e]); p1[j] = pk_bf16(cacc[c2][e + 2], cacc[c2 + 2][e + 2]);
        PIN("+v"(p0), "+v"(p1));
      }
      if constexpr (sb >= 68 && sb < 72) {
        auto r = __builtin_amdgcn_permlane32_swap(p0[sb - 68], p1[sb - 68], false, false);
        p0[sb - 68] = r[0]; p1[sb - 68] = r[1];
        PIN("+v"(p0), "+v"(p1));
      }
      if constexpr (sb == 179) {
        if (tid == 0) __builtin_amdgcn_raw_buffer_store_b32(epoch, wsr, WS_YFL + bid * 4, 0, 16);
      }
      if constexpr (sb == BAS) {
        asm volatile("s_waitcnt vmcnt(0)" : "+v"(yv[0]), "+v"(yv[1]), "+v"(yv[2]), "+v"(yv[3]) :: "memory");
#pragma unroll
        for (int i = 0; i < 2; ++i) {
          auto rx = __builtin_amdgcn_permlane32_swap(x2[i][0], x2[i][2], false, false);
          auto ry = __builtin_amdgcn_permlane32_swap(x2[i][1], x2[i][3], false, false);
          xv[2 * i] = (u32x2){rx[0], ry[0]}; xv[2 * i + 1] = (u32x2){rx[1], ry[1]};
        }
        for (unsigned n = 0; !__all(st.y == epoch) && n < SPIN_CAP; ++n) {
          __builtin_amdgcn_s_sleep(2);
          asm volatile("global_load_dwordx2 %0, %1, %2 sc1\n\ts_waitcnt vmcnt(0)" : "=&v"(st) : "v"(WS_ST + (unsigned)(stat_pos(row) * 8)), "s"(ws) : "memory");
        }
        mb = bf_lo(st.x); rs = 1.0f / bf_hi(st.x);
        PIN("+v"(mb), "+v"(rs));
      }
      if constexpr (sb > BAS && sb <= BAS + 72) {
        constexpr int n = (sb - BAS - 1) / 18, j = (sb - BAS - 1) / 9 % 2, ph = (sb - BAS - 1) % 9;
        if constexpr (ph == 0) { ya = bf_lo(xv[n][j]) - mb; PIN("+v"(ya)); }
        if constexpr (ph == 1) { yb = bf_hi(xv[n][j]) - mb; PIN("+v"(yb)); }
        if constexpr (ph == 2) { pa = pk_bf16(ya, yb); ya = bf_lo(pa); yb = bf_hi(pa); PIN("+v"(ya), "+v"(yb)); }
        if constexpr (ph == 3) { ya *= rs; PIN("+v"(ya)); yb *= rs; PIN("+v"(yb)); pa = pk_bf16(ya, yb); PIN("+v"(pa)); }
        if constexpr (ph == 4) { ya = bf_lo(pa) * bf_lo(yv[n][j]); PIN("+v"(ya)); }
        if constexpr (ph == 5) { yb = bf_hi(pa) * bf_hi(yv[n][j]); PIN("+v"(yb)); }
        if constexpr (ph == 6) { pa = pk_bf16(ya, yb); ya = bf_lo(pa); yb = bf_hi(pa); PIN("+v"(ya), "+v"(yb)); }
        if constexpr (ph == 7) { ya += bf_lo(xv[n][j]); PIN("+v"(ya)); yb += bf_hi(xv[n][j]); PIN("+v"(yb)); }
        if constexpr (ph == 8) { xv[n][j] = pk_bf16(ya, yb); PIN("+v"(xv[n])); }
      }
      if constexpr (sb == 305) { PIN("+v"(yfl)); spin_flag(yfl, ws, vo_yfl, epoch); }
      if constexpr (sb >= 385 && sb < 389) *(LAS f32x4*)(xb + wid * 4096 + (sb - 385) * 1024) = quad<(sb - 385) & 3>(ac2[3]);
      if constexpr (sb == 392) { asm volatile("s_waitcnt lgkmcnt(4)\n\ts_barrier" ::: "memory"); STAMP_AT(32); }
      if constexpr (sb >= 393 && sb < 397) xs[sb - 393] = *(LAS f32x4*)(xb + (wid ^ 4) * 4096 + (sb - 393) * 1024);
      if constexpr (sb >= 398 && sb < 406) {
        constexpr int i = (sb - 398) >> 1;
        if constexpr (sb & 1) __builtin_amdgcn_raw_buffer_store_b128(__builtin_bit_cast(u32x4, xs[i]), wsr, xo + bid * 32768 + i * 1024, 0, 16);
        else { for (int e = 0; e < 4; ++e) { xs[i][e] += ac2[2][4 * i + e]; PIN("+v"(xs[i])); } }
      }
      if constexpr (sb == XFS) {
        wait_v<0>();
        if (lane == 0) __builtin_amdgcn_raw_buffer_store_b32(epoch, wsr, WS_XF + (bid * 8 + wid) * 4, 0, 16);
        STAMP_AT(33);
      }
      if constexpr (sb == 447) gload4s(xf, ws, vo_xf);
      SCHED;
    });
  });
  STAMP_AT(14);
  asm volatile("s_nop 7\n\ts_nop 7" ::: "memory");
  static_for<0, 4>([&](auto G) { *(LAS f32x4*)(xb + (wid ^ 4) * 4096 + G * 1024) = quad<G>(ac2[1]); });
  asm volatile("s_waitcnt vmcnt(0)" : "+v"(xf) :: "memory");
  STAMP_AT(17);
  spin_flag(xf, ws, vo_xf, epoch);
  STAMP_AT(18);
  {
    const char* xp = ws + (size_t)((bid ^ 4) * 32768);
    gload16s<0>(xs[0], xp, xo); gload16s<1024>(xs[1], xp, xo); gload16s<2048>(xs[2], xp, xo); gload16s<3072>(xs[3], xp, xo);
    WAIT_L0;
    BAR;
    f32x4 o[4];
    static_for<0, 4>([&](auto G) { o[G] = quad<G>(ac2[0]) + *(LAS f32x4*)(xb + wid * 4096 + G * 1024); });
    asm volatile("s_waitcnt vmcnt(0)" : "+v"(xs[0]), "+v"(xs[1]), "+v"(xs[2]), "+v"(xs[3]), "+v"(o[0]), "+v"(o[1]), "+v"(o[2]), "+v"(o[3]) :: "memory");
    STAMP_AT(19);
    const float rs2 = rs * rs;
#pragma unroll
    for (int n = 0; n < 4; ++n) {
      const f32x4 b = (o[n] + xs[n]) * rs2;
      const f32x2 lo = rbf2((f32x2){b[0], b[1]}) + (f32x2){bf_lo(xv[n].x), bf_hi(xv[n].x)}, hi = rbf2((f32x2){b[2], b[3]}) + (f32x2){bf_lo(xv[n].y), bf_hi(xv[n].y)};
      xv[n] = (u32x2){pk_bf16(lo.x, lo.y), pk_bf16(hi.x, hi.y)};
    }
#pragma unroll
    for (int n = 0; n < 4; n += 2) {
      auto rx = __builtin_amdgcn_permlane32_swap(xv[n].x, xv[n + 1].x, false, false);
      auto ry = __builtin_amdgcn_permlane32_swap(xv[n].y, xv[n + 1].y, false, false);
      *(u32x4*)(out + (size_t)row * D_ + col + 8 * (n + (fq >> 1))) = (u32x4){rx[0], ry[0], rx[1], ry[1]};
    }
  }
  STAMP_AT(15);
  if (bid == 0 && wid == 0) {
    u32x4 f;
    for (unsigned n = 0; n < SPIN_CAP; ++n) {
      asm volatile("global_load_dwordx4 %0, %1, %2 sc1\n\ts_waitcnt vmcnt(0)" : "=&v"(f) : "v"(WS_KVF + (unsigned)(lane * 16)), "s"(ws) : "memory");
      if (__all(f.x == epoch && f.y == epoch && f.z == epoch && f.w == epoch)) break;
      __builtin_amdgcn_s_sleep(2);
    }
    if (lane == 0) __builtin_amdgcn_raw_buffer_store_b32(ev + 1u, wsr, WS_E, 0, 16);
  }
  STAMP_AT(16);
  STAMP_OUT(38);
}

extern "C" void kernel_launch(void* const* d_in, const int* in_sizes, int n_in, void* d_out, int out_size, void* d_ws, size_t ws_size,
                              hipStream_t stream) {
  k_fused<<<256, 512, 0, stream>>>((const u16*)d_in[0], (const u16*)d_in[1], (const u16*)d_in[2], (const u16*)d_in[3], (const u16*)d_in[4],
                                   (const u16*)d_in[5], (const u16*)d_in[7], (const u16*)d_in[8], (const u16*)d_in[9], (u16*)d_out,
                                   (char*)d_ws + (TAILWS ? (ws_size - WS_END) & ~(size_t)4095 : 0));
}
```

```cpp
#include <hip/hip_runtime.h>
#include <hip/hip_bf16.h>
#include <type_traits>

#define W2W 30
#define W2N 1
typedef unsigned short u16;
typedef short bf16x8 __attribute__((ext_vector_type(8)));
typedef short s16x4 __attribute__((ext_vector_type(4)));
typedef float f32x4 __attribute__((ext_vector_type(4)));
typedef float f32x2 __attribute__((ext_vector_type(2)));
typedef unsigned u32x4 __attribute__((ext_vector_type(4)));
typedef unsigned u32x2 __attribute__((ext_vector_type(2)));
#define LAS __attribute__((address_space(3)))
template <int N> using IC = std::integral_constant<int, N>;

constexpr int S_ = 2048, D_ = 1024, F_ = 3584;

constexpr unsigned WS_E = 0, WS_KVF = 1024, WS_HFL = 2048, WS_YFL = 3072, WS_ST = 4096, WS_XF = 20480, WS_CS = 32768;
constexpr unsigned WS_KVT = 1u << 20, WS_Y2 = 5u << 20, WS_H = 9u << 20, WS_X = 24u << 20, WS_END = 32u << 20;
constexpr unsigned SPIN_CAP = 1u << 18;


__device__ __forceinline__ unsigned pk_bf16(float lo, float hi) {
  typedef __bf16 bf2 __attribute__((ext_vector_type(2)));
  f32x2 v = {lo, hi};
  bf2 r = __builtin_convertvector(v, bf2);
  return __builtin_bit_cast(unsigned, r);
}
__device__ __forceinline__ float bf_lo(unsigned u) { return __uint_as_float(u << 16); }
__device__ __forceinline__ float bf_hi(unsigned u) { return __uint_as_float(u & 0xffff0000u); }
__device__ __forceinline__ float rbf(float x) { return bf_lo(pk_bf16(x, x)); }
__device__ __forceinline__ f32x2 rbf2(f32x2 x) { const unsigned u = pk_bf16(x.x, x.y); return (f32x2){bf_lo(u), bf_hi(u)}; }

#define WAIT_L0 asm volatile("s_waitcnt lgkmcnt(0)" ::: "memory")
#define BAR asm volatile("s_barrier" ::: "memory")
#define SCHED __builtin_amdgcn_sched_barrier(0)
template <int N> __device__ __forceinline__ void wait_v() { asm volatile("s_waitcnt vmcnt(%0)" ::"n"(N) : "memory"); }

__device__ __forceinline__ void glds1(const void* g, unsigned vo, unsigned l) {
  asm volatile("s_mov_b32 m0, %2\n\ts_nop 0\n\tglobal_load_lds_dwordx4 %0, %1" ::"v"(vo), "s"(g), "s"(l) : "memory");
}
__device__ __forceinline__ void glds1_56(const void* g, unsigned vo, unsigned l) {
  asm volatile("s_mov_b32 m0, %2\n\ts_mov_b32 exec_hi, 0x00ffffff\n\tglobal_load_lds_dwordx4 %0, %1\n\ts_mov_b32 exec_hi, -1" ::"v"(vo), "s"(g), "s"(l) : "memory");
}
#define RW 4
#define XROT 1
template <int OFF, class T> __device__ __forceinline__ void gload16n(T& dst, const void* g, unsigned vo) {
  asm volatile("global_load_dwordx4 %0, %1, %2 offset:%3" : "=v"(dst) : "v"(vo), "s"(g), "n"(OFF) : "memory");
}
template <int OFF, class T> __device__ __forceinline__ void gload8n(T& dst, const void* g, unsigned vo) {
  asm volatile("global_load_dwordx2 %0, %1, %2 offset:%3" : "=v"(dst) : "v"(vo), "s"(g), "n"(OFF) : "memory");
}
template <int OFF, class T> __device__ __forceinline__ void gload16(T& dst, const void* g, unsigned vo) {
  asm volatile("global_load_dwordx4 %0, %1, %2 offset:%3" : "=v"(dst) : "v"(vo), "s"(g), "n"(OFF) : "memory");
}
template <int OFF, class T> __device__ __forceinline__ void gload16s(T& dst, const void* g, unsigned vo) {
  asm volatile("global_load_dwordx4 %0, %1, %2 offset:%3 sc1" : "=v"(dst) : "v"(vo), "s"(g), "n"(OFF) : "memory");
}
template <int OFF, class T> __device__ __forceinline__ void gload8(T& dst, const void* g, unsigned vo) {
  asm volatile("global_load_dwordx2 %0, %1, %2 offset:%3" : "=v"(dst) : "v"(vo), "s"(g), "n"(OFF) : "memory");
}
template <int OFF, class T> __device__ __forceinline__ void gload8s(T& dst, const void* g, unsigned vo) {
  asm volatile("global_load_dwordx2 %0, %1, %2 offset:%3 sc1" : "=v"(dst) : "v"(vo), "s"(g), "n"(OFF) : "memory");
}
__device__ __forceinline__ void gload4s(unsigned& dst, const void* g, unsigned vo) {
  asm volatile("global_load_dword %0, %1, %2 sc1" : "=v"(dst) : "v"(vo), "s"(g) : "memory");
}
__device__ __forceinline__ s16x4 tr_read(LAS unsigned char* p) { return __builtin_amdgcn_ds_read_tr16_b64_v4i16((LAS s16x4*)p); }
__device__ __forceinline__ bf16x8 tr_frag(LAS unsigned char* p, int hi_off) {
  const s16x4 lo = tr_read(p), hi = tr_read(p + hi_off);
  return __builtin_shufflevector(lo, hi, 0, 1, 2, 3, 4, 5, 6, 7);
}
template <int CTRL> __device__ __forceinline__ float dpp_add(float x) {
  return x + __uint_as_float(__builtin_amdgcn_update_dpp(0u, __float_as_uint(x), CTRL, 0xf, 0xf, true));
}
__device__ __forceinline__ float wave_sum(float x) {
  x = dpp_add<0xB1>(x); x = dpp_add<0x4E>(x); x = dpp_add<0x141>(x); x = dpp_add<0x140>(x);
  auto r = __builtin_amdgcn_permlane16_swap(__float_as_uint(x), __float_as_uint(x), false, false);
  x = __uint_as_float(r[0]) + __uint_as_float(r[1]);
  auto t = __builtin_amdgcn_permlane32_swap(__float_as_uint(x), __float_as_uint(x), false, false);
  return __uint_as_float(t[0]) + __uint_as_float(t[1]);
}
__device__ __forceinline__ void spin_flag(unsigned& f, const char* base, unsigned vo, unsigned epoch) {
  for (unsigned n = 0; !__all(f == epoch) && n < SPIN_CAP; ++n) {
    __builtin_amdgcn_s_sleep(2);
    asm volatile("global_load_dword %0, %1, %2 sc1\n\ts_waitcnt vmcnt(0)" : "=&v"(f) : "v"(vo), "s"(base) : "memory");
  }
}

template <bool FIRST> __device__ __forceinline__ void mfma1(f32x4& c, const bf16x8& b, const bf16x8& a) {
  if constexpr (FIRST) asm volatile("v_mfma_f32_16x16x32_bf16 %0, %1, %2, 0" : "=&v"(c) : "v"(b), "v"(a));
  else asm volatile("v_mfma_f32_16x16x32_bf16 %0, %1, %2, %0" : "+v"(c) : "v"(b), "v"(a));
}
#define PIN(...) asm volatile("" : __VA_ARGS__)
template <int I, int N, class F> __device__ __forceinline__ void static_for(F&& f) {
  if constexpr (I < N) { f(IC<I>{}); static_for<I + 1, N>(f); }
}
#define KVL 4
#define XW 1
#define KVS 17
#define KVS0 15
#define CBAR 111
#define XFE 424
#define XFC 434
#define BA2 406
#define BAR2 2
#define CSW 9
#define HOW 24
#define BLD 3
#define CPUB 20
#define CCHK 94
#define CSTR 3
#define BAS 354
#define XFS 418
constexpr int g1_dma(int w) { return (w <= 27 ? 2 : 0) + (w <= 26 ? 2 : 0); }
constexpr int g1_kv(int u, int k) {
  const int w = u - KVL;
  if (w < 0 || w > 7) return -1;
  if (k == 10) return w < 4 ? w : w + 3;
  if (k == 23) return w < 2 ? 11 + w : w >= 4 && w < 7 ? w : -1;
  return -1;
}
constexpr int g1_extra(int w) {
  return (w == XW ? 3 : 0) + (w == XW + 5 ? 1 : 0) + (g1_kv(w, 10) >= 0) + (g1_kv(w, 23) >= 0) +
         (w == KVS ? 1 : 0) + (w == KVS0 ? 1 : 0) + (w == CSW ? 1 : 0) + (w == HOW ? 5 : 0) + (w == HOW + 3 ? 2 : 0) + (w == 28 ? 4 : 0) + (w >= W2W && w < W2W + W2N ? 1 : 0);
}
__device__ __forceinline__ int stat_pos(int r) { return (r & ~63) | ((r & 15) << 2) | ((r >> 4) & 3); }

constexpr int G1_ASL = 32768, G1_BOFF = 3 * G1_ASL, G1_UPOFF = 7168 + 16, G1_BSL = 2 * 7168 + 16;
constexpr int CV_CS = 4384;
constexpr int CV_CH = 8 * CV_CS + 4352;
constexpr int L_CS = 4 * CV_CH, L_CNT = L_CS + 896;
constexpr int G2_R = 5, G2_NU = F_ / 256;
constexpr int LDS_BYTES = 163840;
static_assert(G1_BOFF + 4 * G1_BSL <= L_CS && L_CNT + 4 <= LDS_BYTES, "lds map");
constexpr int g2_slot(int u) { return (u + 2) % G2_R * 32768; }
constexpr int G2_T0 = 4 * (G2_NU - 4);
constexpr int g2_u(int t) { return t < G2_T0 ? t >> 2 : G2_NU - 4 + (((t - G2_T0) & 7) >> 1); }
constexpr int g2_sub(int t) { return t < G2_T0 ? (t >> 1) & 1 : t & 1; }
constexpr int g2_h(int t) { return t < G2_T0 ? t & 1 : t < G2_T0 + 8; }
#define A4S 5
#define G2_GAP 3
constexpr int G2_YS = 72;
constexpr int g2_nat(int s) {
  const int u = s >> 5, k = s & 31;
  if (s >= 276 && s <= 288 && (s & 3) == 0) return 100 + 4 * 13 + ((s - 276) >> 2);
  if (s < 16) return s & 1 ? (s < 8 ? 200 + 4 * 4 : 100 + 4 * 2) + (s >> 1) : 0;
  if (u <= 8 && k >= 18 && (k & 3) == 2) return 100 + 4 * (u + 4) + ((k - 18) >> 2);
  if (u <= 9 && k < 16 && (k & 3) == 2) return 200 + 4 * (u + 4) + (k >> 2);
  if (s == G2_YS || s == G2_YS + 4) return 2 + (s - G2_YS) / 4;
  if (s == 240) return 1;
  if (s >= 306 && s <= 318 && (s & 3) == 2) return 10 + ((s - 306) >> 2);
  return 0;
}
struct G2Tab { short op[448]; };
constexpr G2Tab g2_make() {
  G2Tab t{};
  int prev = A4S - G2_GAP;
  for (int s = 0; s < 448; ++s) {
    const int o = g2_nat(s);
    if (!o) continue;
    int at = s > prev + G2_GAP ? s : prev + G2_GAP;
    if (o >> 2 == 25 + 3 && at < 17) at = 17;
    t.op[at] = (short)o;
    prev = at;
  }
  return t;
}
constexpr G2Tab G2T = g2_make();
constexpr int g2_op(int s) { return G2T.op[s]; }
constexpr int g2_at(int o) { for (int i = 0; i < 448; ++i) if (g2_op(i) == o) return i; return -1; }
constexpr int g2_after(int u, int s) {
  int c = u < 3 ? 4 * (3 - u) : 0;
  for (int i = 0; i <= s; ++i) { const int o = g2_op(i); c = (o >= 100 && (o % 100) >> 2 == u) ? 0 : c + (o != 0); }
  return c;
}
constexpr int g2_bar(int s) {
  return s < 304 ? ((s & 31) == 16 ? (s >> 5) + 1 : 0) : s == 304 ? 11 : s == 336 ? 12 : s == 352 ? 13 : 0;
}

constexpr bool g2_ok() {
  for (int s = 0; s < 448; ++s)
    if (g2_bar(s) > 2 && (g2_at(100 + 4 * g2_bar(s) + 3) > s - 24 || g2_at(200 + 4 * g2_bar(s) + 3) > s - 24)) return false;
  return g2_at(3) < g2_at(100 + 4 * 6 + 3) && g2_at(1) > 179 + 32 && g2_at(1) < g2_at(100 + 4 * 11 + 3) && g2_at(10) > 305 && g2_at(13) < BAS - 16;
}
static_assert(g2_ok(), "schedule");

__global__ __launch_bounds__(512, 2) void k_fused(const u16* __restrict__ x, const u16* __restrict__ kin, const u16* __restrict__ vin,
                                                  const u16* __restrict__ shift_k, const u16* __restrict__ ssm_k, const u16* __restrict__ ssm_D,
                                                  const u16* __restrict__ w1, const u16* __restrict__ w3, const u16* __restrict__ w2,
                                                  u16* __restrict__ out, char* __restrict__ ws) {
  __shared__ __attribute__((aligned(16))) unsigned char smem[LDS_BYTES];
  LAS unsigned char* lds = (LAS unsigned char*)smem;
  const int tid = threadIdx.x, lane = tid & 63, wid = __builtin_amdgcn_readfirstlane(tid >> 6);
  const unsigned ldsb = (unsigned)(size_t)lds;
  const int bid = blockIdx.x, xcd = bid & 7, idx = bid >> 3;
  const int fr = lane & 15, fq = lane >> 4, tq = fr >> 2, tp = lane & 3;
  const __amdgpu_buffer_rsrc_t wsr = __builtin_amdgcn_make_buffer_rsrc(ws, 0, WS_END, 0x00020000);

  const int wr = wid & 3, wc = wid >> 2;
  const int pm = idx & 7, pn = xcd * 4 + (idx >> 3);
  const char* gX = (const char*)x + (size_t)pm * (256 * D_ * 2);
  const unsigned voA0 = (unsigned)((wid * 16 + (lane >> 3)) * (D_ * 2) + (((lane & 7) ^ (lane >> 4)) * 16));
  const unsigned voA1 = (unsigned)((wid * 16 + 8 + (lane >> 3)) * (D_ * 2) + (((lane & 7) ^ (4 + (lane >> 4))) * 16));
  const int Q = (wid & 4) | ((wid & 1) << 1) | ((wid >> 1) & 1);
  const int lrow = lane / 14, lch = lane - lrow * 14;
  const unsigned voB = (unsigned)((Q * 4 + lrow) * (F_ * 2) + pn * 224 + lch * 16);
  const int jrot = 16 / RW * (pm % RW) + XROT * xcd;
  auto stageA = [&](int j, int half, int piece) {
    const char* g = gX + half * (128 * D_ * 2) + ((j + jrot) & 15) * 128;
    const unsigned l = ldsb + (j % 3) * G1_ASL + half * 16384 + wid * 2048 + piece * 1024;
    glds1(g, piece ? voA1 : voA0, l);
  };
  auto stageB = [&](int u, int mat) {
    const char* g = (const char*)(mat ? w3 : w1) + (size_t)(unsigned)(((u + 2 * jrot) & 31) * (32 * F_ * 2));
    const unsigned l = ldsb + G1_BOFF + (u & 3) * G1_BSL + wid * 896 + mat * G1_UPOFF;
    glds1_56(g, voB, l);
  };
  stageA(0, 0, 0); stageA(0, 0, 1); stageB(0, 0); stageB(0, 1); stageA(0, 1, 0); stageA(0, 1, 1);
  if (tid == 0) *(LAS unsigned*)(lds + L_CNT) = 0u;

  unsigned ev, epoch = 0, junk2 = 0;
  u32x4 xr[2], sk[2];
  u32x2 kr[7], vr[4], P[4];
  float fa = 0.f, fb_ = 0.f, st_mean = 0.f, kk[7], tt[4], oo[4];
  const int s0 = (bid >> 4) * 128 + (wid >> 1) * 32 + ((lane >> 5) | (((lane >> 3) & 3) << 1)) * 4, d0 = (bid & 15) * 64 + (wid & 1) * 32 + (lane & 7) * 4;
  unsigned kvf, chk = 0;
  u32x4 sg[2], cg[2], h0[2], h1[2], uu[2];
  const int ch0 = bid * 4;
  const int rot = wr == 3 ? 0 : wr;
  f32x4 cs0 = {0.f, 0.f, 0.f, 0.f}, cs1 = cs0, cs2 = cs0;
  f32x2 csv;
  auto filler = [&](auto UC, auto KC) {
    constexpr int u = decltype(UC)::value, k = decltype(KC)::value, T = u * 28 + k;
    auto xe = [&](int i) { const unsigned w = xr[i >> 3][(i >> 1) & 3]; return (i & 1) ? bf_hi(w) : bf_lo(w); };
    auto red = [&](auto SC) {
      constexpr int st = decltype(SC)::value;
      if constexpr (st == 0) fa = dpp_add<0xB1>(fa + fb_);
      if constexpr (st == 1) fa = dpp_add<0x4E>(fa);
      if constexpr (st == 2) fa = dpp_add<0x141>(fa);
      if constexpr (st == 3) fa = dpp_add<0x140>(fa);
      if constexpr (st == 4) { auto r = __builtin_amdgcn_permlane16_swap(__float_as_uint(fa), __float_as_uint(fa), false, false); fa = __uint_as_float(r[0]) + __uint_as_float(r[1]); }
      if constexpr (st == 5) { auto r = __builtin_amdgcn_permlane32_swap(__float_as_uint(fa), __float_as_uint(fa), false, false); fa = (__uint_as_float(r[0]) + __uint_as_float(r[1])) * (1.0f / 1024.0f); }
      PIN("+v"(fa));
    };
    if constexpr (u == XW && k == 9) gload4s(ev, ws, WS_E);
    if constexpr (u == XW && k == 12) gload16<0>(xr[0], x, (unsigned)((bid * 8 + wid) * (D_ * 2) + lane * 16));
    if constexpr (u == XW && k == 17) gload16<1024>(xr[1], x, (unsigned)((bid * 8 + wid) * (D_ * 2) + lane * 16));
    constexpr int ts = T - ((XW + 3) * 28 + 7);
    if constexpr (ts == 0) {
      PIN("+v"(ev), "+v"(xr[0]), "+v"(xr[1]));
      ev = __builtin_amdgcn_readfirstlane(ev);
      PIN("+s"(ev));
      unsigned e = ev * 0x9E3779B1u + 0x7F4A7C15u;
      e ^= e >> 15; e *= 0x85EBCA6Bu; e ^= e >> 13;
      epoch = e;
    }
    if constexpr (ts >= 1 && ts <= 16) {
      if constexpr (ts & 1) { fa += xe(ts - 1); PIN("+v"(fa)); } else { fb_ += xe(ts - 1); PIN("+v"(fb_)); }
    }
    if constexpr (ts >= 17 && ts <= 22) red(IC<ts - 17>{});
    if constexpr (ts == 23) { st_mean = fa; fa = 0.f; fb_ = 0.f; PIN("+v"(st_mean), "+v"(fa), "+v"(fb_), "+v"(xr[0]), "+v"(xr[1])); }
    if constexpr (ts >= 24 && ts <= 39) {
      const float d = xe(ts - 24) - st_mean;
      if constexpr (ts & 1) { fa = __builtin_fmaf(d, d, fa); PIN("+v"(fa)); } else { fb_ = __builtin_fmaf(d, d, fb_); PIN("+v"(fb_)); }
    }
    if constexpr (ts >= 40 && ts <= 45) red(IC<ts - 40>{});
    if constexpr (ts == 46) { fa = rbf(fa) + 1e-5f; PIN("+v"(fa)); }
    if constexpr (ts == 47) { fa = __builtin_amdgcn_sqrtf(rbf(fa)); PIN("+v"(fa)); }
    if constexpr (u == XW + 5 && k == 12) {
      if (lane == 0) __builtin_amdgcn_raw_buffer_store_b64((u32x2){pk_bf16(st_mean, fa), epoch}, wsr, WS_ST + stat_pos(bid * 8 + wid) * 8, 0, 16);
    }
    if constexpr (g1_kv(u, k) >= 0) {
      const unsigned vo = (unsigned)((s0 * D_ + d0) * 2), vp = s0 ? vo - 3 * D_ * 2 : vo;
      constexpr int i = g1_kv(u, k);
      if constexpr (i == 0) gload8n<0>(kr[0], kin, vp);
      if constexpr (i == 1) gload8n<2048>(kr[1], kin, vp);
      if constexpr (i == 2) gload8n<0>(kr[2], kin + 2 * D_, vp);
      if constexpr (i == 3) gload8n<0>(kr[3], kin, vo);
      if constexpr (i == 4) gload8n<2048>(kr[4], kin, vo);
      if constexpr (i == 5) gload8n<0>(kr[5], kin + 2 * D_, vo);
      if constexpr (i == 6) gload8n<2048>(kr[6], kin + 2 * D_, vo);
      if constexpr (i == 7) gload8n<0>(vr[0], vin, vo);
      if constexpr (i == 8) gload8n<2048>(vr[1], vin, vo);
      if constexpr (i == 9) gload8n<0>(vr[2], vin + 2 * D_, vo);
      if constexpr (i == 10) gload8n<2048>(vr[3], vin + 2 * D_, vo);
      if constexpr (i == 11) gload16<0>(sk[0], shift_k, (unsigned)(d0 * 8));
      if constexpr (i == 12) gload16<16>(sk[1], shift_k, (unsigned)(d0 * 8));
    }
    constexpr int tk = T - ((KVL + 10) * 28 + 7);
    if constexpr (tk == 0) PIN("+v"(kr[0]), "+v"(kr[1]), "+v"(kr[2]), "+v"(kr[3]), "+v"(kr[4]), "+v"(kr[5]), "+v"(kr[6]),
                               "+v"(vr[0]), "+v"(vr[1]), "+v"(vr[2]), "+v"(vr[3]), "+v"(sk[0]), "+v"(sk[1]));
    if constexpr (tk >= 0 && tk < 3) { if (s0 == 0) kr[tk] = (u32x2){0u, 0u}; PIN("+v"(kr[tk])); }
    if constexpr (tk >= 3 && tk < 3 + 76) {
      constexpr int c = (tk - 3) / 38 + (tk - 3) / 19 % 2 * 2, j = (tk - 3) % 19, jj = c >> 1;
      auto un = [&](unsigned w) { return (c & 1) ? bf_hi(w) : bf_lo(w); };
      if constexpr (j < 3) { kk[2 * j] = un(kr[2 * j][jj]); kk[2 * j + 1] = un(kr[2 * j + 1][jj]); PIN("+v"(kk[2 * j]), "+v"(kk[2 * j + 1])); }
      if constexpr (j == 3) { kk[6] = un(kr[6][jj]); PIN("+v"(kk[6])); }
      if constexpr (j == 4 || j == 5) {
        const unsigned w = sk[jj][2 * (c & 1) + j - 4];
        tt[2 * (j - 4)] = bf_lo(w); tt[2 * (j - 4) + 1] = bf_hi(w); PIN("+v"(tt[2 * (j - 4)]), "+v"(tt[2 * (j - 4) + 1]));
      }
      if constexpr (j >= 6 && j < 18) {
        constexpr int i = (j - 6) / 3, ph = (j - 6) % 3;
        if constexpr (ph == 0) oo[i] = __builtin_fmaf(kk[i + 1], tt[2], kk[i] * tt[3]);
        if constexpr (ph == 1) oo[i] = __builtin_fmaf(kk[i + 3], tt[0], __builtin_fmaf(kk[i + 2], tt[1], oo[i]));
        if constexpr (ph == 2) oo[i] *= un(vr[i][jj]);
        PIN("+v"(oo[i]));
      }
      if constexpr (j == 18) { P[c] = (u32x2){pk_bf16(oo[3], oo[2]), pk_bf16(oo[1], oo[0])}; PIN("+v"(P[c])); }
    }
    static_assert(KVS0 * 28 + 22 > (KVL + 10) * 28 + 7 + 3 + 37 && KVS0 <= KVS && CSW >= 32 / RW, "kv schedule");
    if constexpr ((u == KVS && k == 7) || (u == KVS0 && k == 22)) {
      constexpr int c = k == 7 ? 1 : 0;
      auto rx = __builtin_amdgcn_permlane32_swap(P[c].x, P[c + 2].x, false, false);
      auto ry = __builtin_amdgcn_permlane32_swap(P[c].y, P[c + 2].y, false, false);
      __builtin_amdgcn_raw_buffer_store_b128((u32x4){rx[1], ry[1], rx[0], ry[0]}, wsr,
                                             WS_KVT + d0 * 4096 + (s0 >> 5) * 256 + c * 128 + (lane >> 5) * 64 + (s0 & 24) * 2, 0, 16);
    }
    if constexpr (u == KVS + 3 && k == 7) {
      if (tid == 0) __builtin_amdgcn_raw_buffer_store_b32(epoch, wsr, WS_KVF + ((bid & 15) * 16 + (bid >> 4)) * 4, 0, 16);
    }
    if constexpr (u == CSW && k == 7) {
      const f32x4 c = wr == 3 ? cs2 : (fr & 2) ? cs1 : cs0;
      csv = (fr & 1) ? (f32x2){c[2], c[3]} : (f32x2){c[0], c[1]};
      PIN("+v"(csv));
    }
    if constexpr (u == CSW && k == 12) {
      if (fr < (wr < 3 ? 4 : 2))
        __builtin_amdgcn_raw_buffer_store_b128((u32x4){__float_as_uint(csv.x), epoch, __float_as_uint(csv.y), epoch}, wsr,
                                               WS_CS + (pn * 8 + pm) * 1792 + (wc * 7 + wr * 2 + (fr >> 1)) * 128 + fq * 32 + (fr & 1) * 16, 0, 16);
    }
    if constexpr (u >= W2W && u < W2W + W2N && k == 9) {
      constexpr int nl = 28 / (W2N ? W2N : 1);
      asm volatile("global_load_dword %0, %1, %2" : "+v"(junk2) : "v"((unsigned)(((bid * 8 + wid) * nl + (lane < nl ? lane : nl - 1)) * 128)),
                   "s"((const char*)w2 + (size_t)(u - W2W) * (2048 * nl * 128)) : "memory");
    }
    if constexpr (W2W < 28 && u == W2W + W2N + 2 && k == 7) PIN("+v"(junk2));
    if constexpr (u == HOW || u == HOW + 3) {
      const unsigned vo_kvf = WS_KVF + (unsigned)((bid >> 4) * 64 + fr * 4);
      const unsigned vo_sg = WS_ST + (unsigned)((pm * 256 + wr * 64 + fr * 4) * 8);
      const unsigned vo_cg = WS_CS + (unsigned)((pn * 8 + (tid & 7)) * 1792 + ((tid >> 3) < 56 ? (tid >> 3) : 55) * 16);
      if constexpr (u == HOW && k == 10) gload16s<0>(sg[0], ws, vo_sg);
      if constexpr (u == HOW && k == 12) gload16s<16>(sg[1], ws, vo_sg);
      if constexpr (u == HOW && k == 17) gload16s<0>(cg[0], ws, vo_cg);
      if constexpr (u == HOW && k == 23) gload16s<0>(cg[1], ws + 896, vo_cg);
      if constexpr (u == HOW && k == 26) gload4s(kvf, ws, vo_kvf);
      if constexpr (u == HOW + 3 && k == 7) { PIN("+v"(kvf), "+v"(sg[0]), "+v"(sg[1]), "+v"(cg[0]), "+v"(cg[1])); chk = kvf ^ epoch; PIN("+v"(chk)); }
      if constexpr (u == HOW + 3 && k == 8) { chk |= (sg[0].y ^ epoch) | (sg[0].w ^ epoch); PIN("+v"(chk)); }
      if constexpr (u == HOW + 3 && k == 9) { chk |= (sg[1].y ^ epoch) | (sg[1].w ^ epoch); PIN("+v"(chk)); }
      if constexpr (u == HOW + 3 && k == 10) { chk |= (cg[0].y ^ epoch) | (cg[0].w ^ epoch); PIN("+v"(chk)); }
      if constexpr (u == HOW + 3 && k == 11) { chk |= (cg[1].y ^ epoch) | (cg[1].w ^ epoch); PIN("+v"(chk)); }
      if constexpr (u == HOW + 3 && k == 12) {
        for (unsigned n = 0; !__all(chk == 0u) && n < SPIN_CAP; ++n) {
          __builtin_amdgcn_s_sleep(2);
          asm volatile("global_load_dwordx4 %0, %5, %8 sc1\n\tglobal_load_dwordx4 %1, %5, %8 offset:16 sc1\n\t"
                       "global_load_dwordx4 %2, %6, %8 sc1\n\tglobal_load_dwordx4 %3, %6, %9 sc1\n\tglobal_load_dword %4, %7, %8 sc1\n\ts_waitcnt vmcnt(0)"
                       : "=&v"(sg[0]), "=&v"(sg[1]), "=&v"(cg[0]), "=&v"(cg[1]), "=&v"(kvf)
                       : "v"(vo_sg), "v"(vo_cg), "v"(vo_kvf), "s"(ws), "s"(ws + 896) : "memory");
          chk = (kvf ^ epoch) | (sg[0].y ^ epoch) | (sg[0].w ^ epoch) | (sg[1].y ^ epoch) | (sg[1].w ^ epoch) |
                ((cg[0].y ^ epoch) | (cg[0].w ^ epoch) | (cg[1].y ^ epoch) | (cg[1].w ^ epoch));
        }
      }
    }
    if constexpr (u == HOW + 3 || u == 28) {
      const int n = tid & 255;
      const unsigned vo = (unsigned)((tid >> 8) * 4096 + n * 16), vp = n > 0 ? vo - 16u : vo;
      const unsigned vu = (unsigned)((n >> 2) * 256 + (tid >> 8) * 128 + (n & 3) * 16);
      const char* hb = (const char*)ssm_k + (size_t)ch0 * 4096;
      const char* ub = ws + WS_KVT + (size_t)ch0 * 4096;
      if constexpr (u == 28 && k == 12) gload16n<0>(h1[0], hb, vo);
      if constexpr (u == 28 && k == 20) gload16n<0>(h0[0], hb, vp);
      if constexpr (u == 28 && k == 16) gload16n<0>(h1[1], hb + 8192, vo);
      if constexpr (u == 28 && k == 24) gload16n<0>(h0[1], hb + 8192, vp);
      if constexpr (u == HOW + 3 && k == 14) gload16s<0>(uu[0], ub, vu);
      if constexpr (u == HOW + 3 && k == 18) gload16s<64>(uu[1], ub, vu);
    }
    if constexpr (u == HOW + 3 && k >= 20) {
      constexpr int i = (k - 20) >> 2, j = k & 3;
      if constexpr (j == 0) { fa = dpp_add<0xB1>(__uint_as_float(cg[i].x)); fb_ = dpp_add<0xB1>(__uint_as_float(cg[i].z)); }
      if constexpr (j == 1) { fa = dpp_add<0x4E>(fa); fb_ = dpp_add<0x4E>(fb_); }
      if constexpr (j == 2) { fa = dpp_add<0x141>(fa); fb_ = dpp_add<0x141>(fb_); }
      if constexpr (j < 3) PIN("+v"(fa), "+v"(fb_));
      else if ((tid & 7) == 0 && tid < 448) *(LAS f32x2*)(lds + L_CS + i * 448 + tid) = (f32x2){fa, fb_};
    }
  };

  int aoff = wr * 8192 + fr * 128 + ((fq ^ (fr >> 1)) * 16);
  const int brow = G1_BOFF + 896 * (4 * (fq >> 1) + (fq & 1)) + 224 * tq + 16 * (7 * wc);
  int boffP[3];
#pragma unroll
  for (int i = 0; i < 3; ++i) boffP[i] = brow + 8 * tp + 32 * (i + rot >= 3 ? i + rot - 3 : i + rot);
  int boffM = brow + 96 + (tp >> 1) * G1_UPOFF + 8 * (tp & 1);
  f32x4 acc[4][7];
  bf16x8 one = {0x3f80, 0x3f80, 0x3f80, 0x3f80, 0x3f80, 0x3f80, 0x3f80, 0x3f80};
  PIN("+v"(one), "+v"(cs0), "+v"(cs1), "+v"(cs2), "+v"(aoff), "+v"(boffM), "+v"(boffP[0]), "+v"(boffP[1]), "+v"(boffP[2]));
  BAR;
  stageB(1, 0); stageB(1, 1); stageA(1, 0, 0); stageA(1, 0, 1);
  stageB(2, 0); stageB(2, 1); stageA(1, 1, 0); stageA(1, 1, 1);
  stageB(3, 0); stageB(3, 1); stageA(2, 0, 0);
  u32x2 dd;
  asm volatile("s_load_dwordx2 %0, %1, 0x0" : "=s"(dd) : "s"(ssm_D + ch0) : "memory");
  wait_v<11 + g1_extra(-1)>();
  asm volatile("s_waitcnt lgkmcnt(0)" : "+s"(dd) :: "memory");
  BAR;
  bf16x8 A[4], B[7];
  int ab = aoff;
  auto readA = [&](int u, int m) { A[m] = *(LAS bf16x8*)(lds + ab + ((u >> 1) % 3 == 1 ? G1_ASL : 0) + m * 2048); };
  auto readB = [&](int u, int n) { B[n] = tr_frag(lds + (u & 3) * G1_BSL + (n == 6 ? boffM : boffP[n >> 1] + (n & 1) * G1_UPOFF), 2 * 896); };
  readA(0, 0);
#pragma unroll
  for (int n = 0; n < 7; ++n) readB(0, n);
  readA(0, 1); readA(0, 2); readA(0, 3);
  SCHED;
  static_for<0, 32>([&](auto UC) {
    constexpr int u = decltype(UC)::value;
    static_for<0, 28>([&](auto KC) {
      constexpr int k = decltype(KC)::value;
      constexpr int n = k < 7 ? k : k < 14 ? 13 - k : (k - 14) >> 1;
      constexpr int m = k < 7 ? 0 : k < 14 ? 1 : 2 + ((k & 1) ^ (n & 1));
      mfma1<u == 0>(acc[m][n], B[n], A[m]);
      if constexpr (k == 2 && u <= 27) stageA((u + 4) >> 1, u & 1, 1);
      if constexpr (k == 6) {
        if constexpr (u <= 30) wait_v<g1_dma(u - 2) + g1_extra(u - 2) + g1_dma(u - 1) + g1_extra(u - 1)>();
        WAIT_L0;
        BAR;
        if constexpr (u < 31) {
          ab = aoff ^ (((u + 1) & 1) * 64) ^ (((u + 1) >> 1) % 3 == 2 ? 2 * G1_ASL : 0);
          PIN("+v"(ab));
          readA(u + 1, 0);
        }
      }
      if constexpr (k == 8 && u <= 27) stageB(u + 4, 0);
      if constexpr (k == 13) {
        if constexpr (u < 32 / RW)
          asm volatile("s_cmp_lg_u32 %7, %8\n\ts_cbranch_scc1 1f\n\t"
                       "v_mfma_f32_16x16x32_bf16 %0, %3, %6, %0\n\tv_mfma_f32_16x16x32_bf16 %1, %4, %6, %1\n\tv_mfma_f32_16x16x32_bf16 %2, %5, %6, %2\n1:"
                       : "+v"(cs0), "+v"(cs1), "+v"(cs2) : "v"(B[0]), "v"(B[1]), "v"(B[6]), "v"(one), "s"(pm / RW), "n"(u >> 2) : "scc");
        if constexpr (u < 31) readA(u + 1, 1);
      }
      if constexpr (k == 14 && u <= 26) stageA((u + 5) >> 1, (u + 1) & 1, 0);
      if constexpr (k == 20 && u <= 27) stageB(u + 4, 1);
      if constexpr (k >= 15 && (k & 1) && u < 31) readB(u + 1, (k - 15) >> 1);
      if constexpr (k == 27 && u < 31) { readA(u + 1, 2); readA(u + 1, 3); }
      filler(UC, KC);
      SCHED;
    });
  });
  asm volatile("s_nop 7\n\ts_nop 7" ::: "memory");

  {
    float mu[4], er[4];
#pragma unroll
    for (int m = 0; m < 4; ++m) { const unsigned w = sg[m >> 1][2 * (m & 1)]; mu[m] = -bf_lo(w); er[m] = -1.44269504089f * __builtin_amdgcn_rcpf(bf_hi(w)); }
    auto act = [&](f32x4 g, f32x4 up, f32x4 cg_, f32x4 cu_, float nm, float e_) {
      g = g + cg_ * nm;
      up = up + cu_ * nm;
      f32x4 e = g * e_;
#pragma unroll
      for (int j = 0; j < 4; ++j) e[j] = __builtin_amdgcn_exp2f(e[j]);
      e = e + 1.0f;
#pragma unroll
      for (int j = 0; j < 4; ++j) e[j] = __builtin_amdgcn_rcpf(e[j]);
      const f32x4 hh = (g * e) * up;
      return (u32x2){pk_bf16(hh[0], hh[1]), pk_bf16(hh[2], hh[3])};
    };
    auto build = [&](int i, int e0, int e1) {
      SCHED;
      const int c = (tid >> 8) + 2 * i, n = tid & 255;
      if (e0 == 0 && n == 0) h0[i] = (u32x4){0u, 0u, 0u, 0u};
      const unsigned d[8] = {h0[i][0], h0[i][1], h0[i][2], h0[i][3], h1[i][0], h1[i][1], h1[i][2], h1[i][3]};
      LAS unsigned char* base = lds + c * CV_CH + (n + 16) * 16;
#pragma unroll
      for (int e = e0; e < e1; ++e) {
        const int b = (e + 1) / 2;
        u32x4 t = uu[i];
        if (e < 8) {
#pragma unroll
          for (int j = 0; j < 4; ++j) t[j] = (e & 1) ? d[b + j] : __builtin_amdgcn_alignbit(d[b + j + 1], d[b + j], 16);
        }
        *(LAS u32x4*)(base + e * CV_CS) = t;
      }
      SCHED;
    };
    {
      const int c = tid >> 7, e = (tid >> 4) & 7, p = tid & 15;
      *(LAS u32x4*)(lds + c * CV_CH + e * CV_CS + p * 16) = (u32x4){0u, 0u, 0u, 0u};
      if (tid < 64) *(LAS u32x4*)(lds + (tid >> 4) * CV_CH + 8 * CV_CS + p * 16) = (u32x4){0u, 0u, 0u, 0u};
    }
    const int fb = pn * 112 + wc * 56;
    const unsigned hb = WS_H + (unsigned)(pm * 16 + wr * 4) * (F_ / 32 * 1024) + fr * 16;
#pragma unroll
    for (int i = 0; i < 3; ++i) {
      const int ir = i + rot >= 3 ? i + rot - 3 : i + rot;
      const f32x4 cgv = *(LAS f32x4*)(lds + L_CS + (wc * 112 + ir * 32 + 4 * fq) * 4), cuv = *(LAS f32x4*)(lds + L_CS + (wc * 112 + ir * 32 + 16 + 4 * fq) * 4);
      const unsigned o = hb + (unsigned)(fq & 1) * (F_ / 32 * 1024) + (unsigned)((fb + ir * 16) / 8 + (fq >> 1)) * 256;
#pragma unroll
      for (int m = 0; m < 4; m += 2) {
        const int q = 2 * i + m / 2 - BLD;
        if (q == 0) asm volatile("s_waitcnt vmcnt(%6)" : "+v"(h0[0]), "+v"(h0[1]), "+v"(h1[0]), "+v"(h1[1]), "+v"(uu[0]), "+v"(uu[1]) : "n"(BLD + (W2W >= 28 && W2N)) : "memory");
        if (q >= 0 && q < 3) build(q * 2 / 3, q * 2 % 3 * 3, q * 2 % 3 * 3 + 3);
        const u32x2 a = act(acc[m][2 * i], acc[m][2 * i + 1], cgv, cuv, mu[m], er[m]);
        if (q >= 0 && q < 3) build((q * 2 + 1) / 3, (q * 2 + 1) % 3 * 3, (q * 2 + 1) % 3 * 3 + 3);
        const u32x2 b = act(acc[m + 1][2 * i], acc[m + 1][2 * i + 1], cgv, cuv, mu[m + 1], er[m + 1]);
        auto rx = __builtin_amdgcn_permlane16_swap(a.x, b.x, false, false);
        auto ry = __builtin_amdgcn_permlane16_swap(a.y, b.y, false, false);
        __builtin_amdgcn_raw_buffer_store_b128((u32x4){rx[0], ry[0], rx[1], ry[1]}, wsr, o + m * (F_ / 32 * 1024), 0, 16);
        SCHED;
      }
    }
    {
      const f32x4 cgv = *(LAS f32x4*)(lds + L_CS + (wc * 112 + 96 + 4 * (fq & 1)) * 4), cuv = *(LAS f32x4*)(lds + L_CS + (wc * 112 + 104 + 4 * (fq & 1)) * 4);
      u32x2 w[2];
#pragma unroll
      for (int m = 0; m < 4; m += 2) {
        f32x4 a = acc[m][6], b = acc[m + 1][6];
#pragma unroll
        for (int j = 0; j < 4; ++j) {
          auto r = __builtin_amdgcn_permlane32_swap(__float_as_uint(a[j]), __float_as_uint(b[j]), false, false);
          a[j] = __uint_as_float(r[0]);
          b[j] = __uint_as_float(r[1]);
        }
        w[m >> 1] = act(a, b, cgv, cuv, (fq >> 1) ? mu[m + 1] : mu[m], (fq >> 1) ? er[m + 1] : er[m]);
      }
      auto rx = __builtin_amdgcn_permlane16_swap(w[0].x, w[1].x, false, false);
      auto ry = __builtin_amdgcn_permlane16_swap(w[0].y, w[1].y, false, false);
      const unsigned o = hb + (unsigned)(2 * (fq & 1) + (fq >> 1)) * (F_ / 32 * 1024) + (unsigned)((fb + 48) / 8) * 256;
      __builtin_amdgcn_raw_buffer_store_b128((u32x4){rx[0], ry[0], rx[1], ry[1]}, wsr, o, 0, 16);
    }
  }
  WAIT_L0;
  BAR;
  const int gk = wid >> 2, kh = xcd >> 2;
  const int pm2 = (xcd & 3) * 4 + (idx & 3);
  const int pn2 = idx >> 2;
  unsigned hfl;
  const unsigned vo_hfl = WS_HFL + (unsigned)(((pm2 >> 1) * 32 + kh * 16 + fr) * 4);
  const char* gA = ws + WS_H + (size_t)(pm2 * 8 + 2 * wr) * (F_ / 32 * 1024) + (kh * 56 + gk * 2) * 1024;
  const unsigned vA[2] = {(unsigned)(lane * 16 + gk * (F_ / 32 * 1024)), (unsigned)(lane * 16 + (gk ^ 1) * (F_ / 32 * 1024))};
  const char* gB = (const char*)w2 + (size_t)kh * (F_ / 2 * D_ * 2) + pn2 * 256;
  unsigned vB[2];
#pragma unroll
  for (int i = 0; i < 2; ++i) vB[i] = (unsigned)((wid * 16 + i * 8 + fq) * (D_ * 2) + (fr ^ ((fq | (i << 2)) << 1)) * 16);
  bf16x8 A2[G2_R][2][2];
  u32x4 Bp[12], x2[2];
  u32x2 st;
  const int pc = pn2 * 2 + kh;
  const int row = pm2 * 128 + wr * 32 + gk * 16 + fr;
  const unsigned vx = (unsigned)((row * D_ + pc * 64 + (fq & 1) * 16 + (fq & 2) * 4) * 2);
  auto stage2B = [&](int u, int i) { glds1(gB + (size_t)(u * 128 + (i & 1) * 4) * (D_ * 2), vB[i >> 1], ldsb + g2_slot(u) + wid * 4096 + i * 1024); };
  auto stage2A = [&](auto UC, auto IC_) {
    constexpr int u = decltype(UC)::value, sub = decltype(IC_)::value >> 1, m = decltype(IC_)::value & 1;
    gload16s<sub * 1024>(A2[u % G2_R][sub][m], gA + u * 4096, vA[m]);
  };

  f32x4 cacc[8];
  u32x2 ur[4];
  auto putB = [&](int i) { *(LAS u32x4*)(lds + g2_slot(i / 4) + wid * 4096 + i % 4 * 1024 + lane * 16) = Bp[i]; };
  auto putC = [&](int t) { *(LAS f32x4*)(lds + (wid * 8 + t) * 1024 + lane * 16) = cacc[t]; };
  {
    const int c = wid & 3, half = wid >> 2;
    LAS unsigned char* cb = lds + c * CV_CH;
    LAS unsigned char* ap = cb + (fr & 7) * CV_CS + (fr >> 3) * 256 + fq * 16 + half * 256;
    LAS unsigned char* bp = cb + 8 * CV_CS + (32 + fr - fq) * 16 - half * 256 - 192;
    bf16x8 Ac[8], Bc[8];
    Bc[0] = *(LAS bf16x8*)(bp + 192);
#pragma unroll
    for (int a = 7; a >= 0; --a) Ac[a] = *(LAS bf16x8*)(ap + a * 512);
#pragma unroll
    for (int a = 1; a < 8; ++a) Bc[a] = *(LAS bf16x8*)(bp + 192 + a * 512);
    SCHED;
    static_for<0, 32>([&](auto RC) {
      constexpr int bb = decltype(RC)::value >> 3, dl = decltype(RC)::value & 7;
      static_for<0, 8 - dl>([&](auto JC) {
        constexpr int a = 7 - dl - decltype(JC)::value, sc = bb * 36 + dl * 8 - dl * (dl - 1) / 2 + decltype(JC)::value;
        mfma1<bb == 0 && dl == 0>(cacc[a + dl], Ac[a], Bc[dl]);
        if constexpr (bb < 3 && a == 7 - dl) Ac[a] = *(LAS bf16x8*)(ap + (bb + 1) * 64 + a * 512);
        if constexpr (bb < 3 && a == 0) Bc[dl] = *(LAS bf16x8*)(bp + (2 - bb) * 64 + dl * 512);
        if constexpr (sc == CPUB) {
          wait_v<0>();
          if constexpr (W2W >= 28) PIN("+v"(junk2));
          if (lane == 0) {
            const unsigned old = __hip_atomic_fetch_add((LAS unsigned*)(lds + L_CNT), 1u, __ATOMIC_RELAXED, __HIP_MEMORY_SCOPE_WORKGROUP);
            if (old == 7u) __builtin_amdgcn_raw_buffer_store_b32(epoch, wsr, WS_HFL + (pm * 32 + pn) * 4, 0, 16);
          }
        }
        if constexpr (sc > CPUB && sc <= CPUB + 36 && (sc - CPUB) % 3 == 0) {
          constexpr int i = (sc - CPUB) / 3 - 1;
          gload16<0>(Bp[i], gB + (size_t)(i / 4 * 128 + (i & 1) * 4) * (D_ * 2), vB[i >> 1 & 1]);
        }
        if constexpr (sc == CPUB + 39) {
          gload16<0>(x2[0], x, vx); gload16<64>(x2[1], x, vx);
          gload8s<0>(st, ws, WS_ST + (unsigned)(stat_pos(row) * 8));
        }
        if constexpr (sc == CCHK - 16) gload4s(hfl, ws, vo_hfl);
        if constexpr (sc == CCHK) {
          asm volatile("s_waitcnt vmcnt(0)" : "+v"(hfl), "+v"(Bp[0]), "+v"(Bp[1]), "+v"(Bp[2]), "+v"(Bp[3]), "+v"(Bp[4]), "+v"(Bp[5]), "+v"(Bp[6]),
                       "+v"(Bp[7]), "+v"(Bp[8]), "+v"(Bp[9]), "+v"(Bp[10]), "+v"(Bp[11]), "+v"(x2[0]), "+v"(x2[1]), "+v"(st) :: "memory");
          spin_flag(hfl, ws, vo_hfl, epoch);
        }
        if constexpr (sc > CCHK && sc <= CCHK + 16 * CSTR && (sc - CCHK) % CSTR == 0) {
          constexpr int i = (sc - CCHK) / CSTR - 1;
          stage2A(IC<i / 4>{}, IC<i % 4>{});
        }
        if constexpr (sc == CBAR) {
          asm volatile("s_waitcnt lgkmcnt(0)\n\ts_barrier" : "+v"(ur[0]), "+v"(ur[1]), "+v"(ur[2]), "+v"(ur[3]), "+v"(Ac[0]), "+v"(Ac[1]), "+v"(Ac[2]),
                       "+v"(Bc[5]), "+v"(Bc[6]), "+v"(Bc[7]) :: "memory");
        }
        if constexpr (sc > CBAR && sc <= CBAR + 24 && (sc - CBAR) % 2 == 0) putB((sc - CBAR) / 2 - 1);
        if constexpr (sc == 120 || sc == 126 || sc == 132 || sc == 137 || sc == 141) putC(sc == 120 ? 0 : sc == 126 ? 1 : sc == 132 ? 2 : sc == 137 ? 3 : 4);
        if constexpr (sc >= 96 && sc < 100) ur[sc - 96] = *(LAS u32x2*)(lds + (sc - 96) * CV_CH + 8 * CV_CS + (32 * wid + 16 * (fq >> 1) + fr + 16) * 16 + ((fq & 1) ? 0 : 8));
        SCHED;
      });
    });
  }
  static_assert(CBAR >= 108 && CBAR < 120 && CCHK < CBAR, "conv schedule");
  asm volatile("s_nop 7\n\ts_nop 7" ::: "memory");
  SCHED;
  putC(5); putC(6); putC(7);
  const float Dv[4] = {bf_lo(dd.x), bf_hi(dd.x), bf_lo(dd.y), bf_hi(dd.y)};
  float ya, yb;
  unsigned pa, pb;
  u32x4 p0, p1;

  int bo[8];
#pragma unroll
  for (int n = 0; n < 8; ++n) { bo[n] = 65536 + (64 * gk + 8 * fq + tq) * 256 + 32 * (n ^ (4 * kh) ^ tq ^ (4 * (fq & 1))) + 8 * tp; PIN("+v"(bo[n])); }
  f32x4 ac2[2][8], xs[4];
  bf16x8 B2[2][4];
  auto read2 = [&](auto TC, auto NC) {
    constexpr int t = decltype(TC)::value, u = g2_u(t), n = 4 * g2_h(t) + decltype(NC)::value;
    constexpr int d = (g2_slot(u) & ~65535) - (g2_slot(u > 0 ? u - 1 : 0) & ~65535);
    if constexpr (g2_sub(t) == 0 && d != 0) { bo[n] += d; PIN("+v"(bo[n])); }
    B2[t & 1][decltype(NC)::value] = tr_frag(lds + bo[n] + (g2_slot(u) & 65535) + g2_sub(t) * 8192, 1024);
  };
  unsigned yfl;
  const unsigned vo_yfl = WS_YFL + (unsigned)(pc * 64 + fr * 4);
  const unsigned xo = WS_X + (unsigned)(wid * 4096 + lane * 16);
  LAS unsigned char* xb = lds + g2_slot(9) + lane * 16;
  u32x2 xv[4], yv[4];
  float mb, rs;
  unsigned xf;
  unsigned late = 0;
  const char* xp = ws + (size_t)((bid ^ 4) * 32768);
  auto getX = [&](int when) {
    asm volatile("s_cmp_lg_u32 %6, 0\n\ts_cbranch_scc%7 1f\n\tglobal_load_dwordx4 %0, %4, %5 sc1\n\tglobal_load_dwordx4 %1, %4, %5 offset:1024 sc1\n\t"
                 "global_load_dwordx4 %2, %4, %5 offset:2048 sc1\n\tglobal_load_dwordx4 %3, %4, %5 offset:3072 sc1\n1:"
                 : "+v"(xs[0]), "+v"(xs[1]), "+v"(xs[2]), "+v"(xs[3]) : "v"(xo), "s"(xp), "s"(late), "n"(when) : "memory", "scc");
  };
  const unsigned vo_xf = WS_XF + (unsigned)(((bid ^ 4) * 8 + wid) * 4);
  asm volatile("s_waitcnt vmcnt(12) lgkmcnt(0)\n\ts_barrier" : "+v"(A2[0][0][0]), "+v"(A2[0][0][1]), "+v"(A2[0][1][0]), "+v"(A2[0][1][1]) :: "memory");
  static_for<0, 4>([&](auto NC) { read2(IC<0>{}, NC); });
  static_for<0, 4>([&](auto NC) { read2(IC<1>{}, NC); });
  SCHED;
  static_for<0, 4 * G2_NU>([&](auto TC) {
    constexpr int t = decltype(TC)::value, u = g2_u(t);
    static_for<0, 8>([&](auto JC) {
      constexpr int j = decltype(JC)::value, sb = t * 8 + j, n = j >> 1, m = (j & 1) ^ (n & 1), op = g2_op(sb), bu = g2_bar(sb);
      if constexpr (sb < 432) mfma1<(t < 2)>(ac2[m][4 * g2_h(t) + n], B2[t & 1][n], A2[u % G2_R][g2_sub(t)][m]);
      else mfma1<false>(ac2[sb < 440][j & 3], B2[j >> 2][j & 3], A2[u % G2_R][j >> 2][sb < 440]);
      if constexpr (sb >= 442 && sb < 446) *(LAS f32x4*)(xb + (wid ^ 4) * 4096 + (sb - 442) * 1024) = ac2[1][sb - 442];
      if constexpr ((j & 1) && t + 2 < 4 * G2_NU) read2(IC<t + 2>{}, IC<n>{});
      if constexpr (op >= 200) stage2A(IC<(op - 200) / 4>{}, IC<op & 3>{});
      else if constexpr (op >= 100) stage2B((op - 100) / 4, op & 3);
      else if constexpr (op == 1) gload4s(yfl, ws, vo_yfl);
      else if constexpr (op == 2 || op == 3)
        __builtin_amdgcn_raw_buffer_store_b128(op == 2 ? p0 : p1, wsr, WS_Y2 + (unsigned)(bid * 16384 + wid * 2048 + 32 * (2 * fr + (fq & 1)) + 16 * (fq >> 1)) + (op - 2) * 1024, 0, 16);
      else if constexpr (op >= 10) gload8s<0>(yv[op - 10], ws + (op - 10) * 65536, WS_Y2 + (unsigned)(((pc * 16 + fq) * S_ + row) * 8));
      if constexpr (bu > 0) {
        constexpr int r0 = bu % G2_R, r1 = (bu == 11 ? 10 : bu) % G2_R;
        if constexpr (bu == 9) WAIT_L0;
        if constexpr (bu == 1) asm volatile("s_waitcnt lgkmcnt(2)" ::: "memory");
        asm volatile("s_waitcnt vmcnt(%4)\n\ts_barrier" : "+v"(A2[r0][0][0]), "+v"(A2[r0][0][1]), "+v"(A2[r0][1][0]), "+v"(A2[r0][1][1])
                     : "n"(g2_after(bu, sb)) : "memory");
        if constexpr (r0 != r1) PIN("+v"(A2[r1][0][0]), "+v"(A2[r1][0][1]), "+v"(A2[r1][1][0]), "+v"(A2[r1][1][1]));
      }
      if constexpr (sb >= 6 && sb < 14) cacc[sb - 6] = *(LAS f32x4*)(lds + (((sb - 6) >> 1) + 4 * (sb & 1)) * 8192 + wid * 1024 + lane * 16);
      if constexpr (sb >= 16 && sb < 64) {
        constexpr int cc = (sb - 16) / 12, e = (sb - 16) / 6 % 2 * 2, ph = (sb - 16) % 6;
        const unsigned uw = e ? ur[cc].x : ur[cc].y;
        if constexpr (ph == 0) { ya = cacc[2 * cc][e] + cacc[2 * cc + 1][e]; PIN("+v"(ya)); yb = cacc[2 * cc][e + 1] + cacc[2 * cc + 1][e + 1]; PIN("+v"(yb)); }
        if constexpr (ph == 1) { pa = pk_bf16(ya, yb); ya = bf_hi(uw); yb = bf_lo(uw); PIN("+v"(pa), "+v"(ya), "+v"(yb)); }
        if constexpr (ph == 2) { ya *= Dv[cc]; PIN("+v"(ya)); yb *= Dv[cc]; PIN("+v"(yb)); }
        if constexpr (ph == 3) { pb = pk_bf16(ya, yb); ya = bf_lo(pa); yb = bf_hi(pa); PIN("+v"(pb), "+v"(ya), "+v"(yb)); }
        if constexpr (ph == 4) { cacc[2 * cc][e] = ya + bf_lo(pb); PIN("+v"(cacc[2 * cc])); }
        if constexpr (ph == 5) { cacc[2 * cc][e + 1] = yb + bf_hi(pb); PIN("+v"(cacc[2 * cc])); }
      }
      if constexpr (sb >= 64 && sb < 68) {
        constexpr int j = sb - 64, e = j >> 1, c2 = (j & 1) * 4;
        p0[j] = pk_bf16(cacc[c2][e], cacc[c2 + 2][e]); p1[j] = pk_bf16(cacc[c2][e + 2], cacc[c2 + 2][e + 2]);
        PIN("+v"(p0), "+v"(p1));
      }
      if constexpr (sb >= 68 && sb < 72) {
        auto r = __builtin_amdgcn_permlane32_swap(p0[sb - 68], p1[sb - 68], false, false);
        p0[sb - 68] = r[0]; p1[sb - 68] = r[1];
        PIN("+v"(p0), "+v"(p1));
      }
      if constexpr (sb == 179) {
        if (tid == 0) __builtin_amdgcn_raw_buffer_store_b32(epoch, wsr, WS_YFL + bid * 4, 0, 16);
      }
      if constexpr (sb == BAS) {
        asm volatile("s_waitcnt vmcnt(0)" : "+v"(yv[0]), "+v"(yv[1]), "+v"(yv[2]), "+v"(yv[3]) :: "memory");
      }
      if constexpr (sb == BA2) {
        PIN("+v"(x2[0]), "+v"(x2[1]), "+v"(st));
#pragma unroll
        for (int i = 0; i < 2; ++i) {
          auto rx = __builtin_amdgcn_permlane16_swap(x2[i][0], x2[i][2], false, false);
          auto ry = __builtin_amdgcn_permlane16_swap(x2[i][1], x2[i][3], false, false);
          xv[2 * i] = (u32x2){rx[0], ry[0]}; xv[2 * i + 1] = (u32x2){rx[1], ry[1]};
        }
        for (unsigned n = 0; !__all(st.y == epoch) && n < SPIN_CAP; ++n) {
          __builtin_amdgcn_s_sleep(2);
          asm volatile("global_load_dwordx2 %0, %1, %2 sc1\n\ts_waitcnt vmcnt(0)" : "=&v"(st) : "v"(WS_ST + (unsigned)(stat_pos(row) * 8)), "s"(ws) : "memory");
        }
        mb = bf_lo(st.x); rs = 1.0f / bf_hi(st.x);
        PIN("+v"(mb), "+v"(rs));
      }
      static_assert(BA2 > BAS && BA2 + 72 / BAR2 <= 448, "output stage");
      if constexpr (sb >= BA2 && sb < BA2 + 72 / BAR2) static_for<0, BAR2>([&](auto RC) {
        constexpr int q = (sb - BA2) * BAR2 + decltype(RC)::value, n = q / 18, j = q / 9 % 2, ph = q % 9;
        if constexpr (ph == 0) { ya = bf_lo(xv[n][j]) - mb; PIN("+v"(ya)); }
        if constexpr (ph == 1) { yb = bf_hi(xv[n][j]) - mb; PIN("+v"(yb)); }
        if constexpr (ph == 2) { pa = pk_bf16(ya, yb); ya = bf_lo(pa); yb = bf_hi(pa); PIN("+v"(ya), "+v"(yb)); }
        if constexpr (ph == 3) { ya *= rs; PIN("+v"(ya)); yb *= rs; PIN("+v"(yb)); pa = pk_bf16(ya, yb); PIN("+v"(pa)); }
        if constexpr (ph == 4) { ya = bf_lo(pa) * bf_lo(yv[n][j]); PIN("+v"(ya)); }
        if constexpr (ph == 5) { yb = bf_hi(pa) * bf_hi(yv[n][j]); PIN("+v"(yb)); }
        if constexpr (ph == 6) { pa = pk_bf16(ya, yb); ya = bf_lo(pa); yb = bf_hi(pa); PIN("+v"(ya), "+v"(yb)); }
        if constexpr (ph == 7) { ya += bf_lo(xv[n][j]); PIN("+v"(ya)); yb += bf_hi(xv[n][j]); PIN("+v"(yb)); }
        if constexpr (ph == 8) { xv[n][j] = pk_bf16(ya, yb); PIN("+v"(xv[n])); }
      });
      if constexpr (sb == 305) { PIN("+v"(yfl)); spin_flag(yfl, ws, vo_yfl, epoch); }
      if constexpr (sb >= 385 && sb < 389) *(LAS f32x4*)(xb + wid * 4096 + (sb - 385) * 1024) = ac2[1][4 + sb - 385];
      if constexpr (sb == 392) { asm volatile("s_waitcnt lgkmcnt(4)\n\ts_barrier" ::: "memory"); }
      if constexpr (sb >= 393 && sb < 397) xs[sb - 393] = *(LAS f32x4*)(xb + (wid ^ 4) * 4096 + (sb - 393) * 1024);
      if constexpr (sb >= 398 && sb < 406) {
        constexpr int i = (sb - 398) >> 1;
        if constexpr (sb & 1) __builtin_amdgcn_raw_buffer_store_b128(__builtin_bit_cast(u32x4, xs[i]), wsr, xo + bid * 32768 + i * 1024, 0, 16);
        else { for (int e = 0; e < 4; ++e) xs[i][e] += ac2[0][4 + i][e]; PIN("+v"(xs[i])); }
      }
      if constexpr (sb == XFS) {
        wait_v<0>();
        if (lane == 0) __builtin_amdgcn_raw_buffer_store_b32(epoch, wsr, WS_XF + (bid * 8 + wid) * 4, 0, 16);
      }
      static_assert(XFS > 405 && XFC >= XFS && XFE < XFC && XFC < 447, "hand-off schedule");
      if constexpr (sb == XFC) {
        asm volatile("s_waitcnt vmcnt(%1)" : "+v"(xf) : "n"(XFE < XFS ? 1 : 0) : "memory");
        late = __builtin_amdgcn_readfirstlane(xf) ^ epoch;
        getX(1);
      }
      if constexpr (sb == XFE) gload4s(xf, ws, vo_xf);
      if constexpr (sb == 447)
        asm volatile("s_cmp_lg_u32 %3, 0\n\ts_cbranch_scc0 1f\n\tglobal_load_dword %0, %1, %2 sc1\n1:" : "+v"(xf) : "v"(vo_xf), "s"(ws), "s"(late) : "memory", "scc");
      SCHED;
    });
  });
  static_assert(g2_u(54) == g2_u(55) && g2_sub(54) == 0 && g2_sub(55) == 1 && g2_h(54) == 0 && g2_h(55) == 0, "last half-steps");
  asm volatile("s_cmp_lg_u32 %1, 0\n\ts_cbranch_scc0 1f\n\ts_waitcnt vmcnt(0)\n1:" : "+v"(xf) : "s"(late) : "memory", "scc");
  spin_flag(xf, ws, vo_xf, epoch);
  {
    getX(0);
    WAIT_L0;
    BAR;
    asm volatile("s_nop 7\n\ts_nop 7" ::: "memory");
    SCHED;
#pragma unroll
    for (int n = 0; n < 4; ++n) ac2[0][n] += *(LAS f32x4*)(xb + wid * 4096 + n * 1024);
    asm volatile("s_waitcnt vmcnt(0)" : "+v"(xs[0]), "+v"(xs[1]), "+v"(xs[2]), "+v"(xs[3]), "+v"(ac2[0][0]), "+v"(ac2[0][1]), "+v"(ac2[0][2]), "+v"(ac2[0][3]) :: "memory");
    const float rs2 = rs * rs;
    const auto outr = __builtin_amdgcn_make_buffer_rsrc(out, 0, S_ * D_ * 2, 0x00020000);
#pragma unroll
    for (int n = 0; n < 4; ++n) {
      const f32x4 b = (ac2[0][n] + xs[n]) * rs2;
      const f32x2 lo = rbf2((f32x2){b[0], b[1]}) + (f32x2){bf_lo(xv[n].x), bf_hi(xv[n].x)}, hi = rbf2((f32x2){b[2], b[3]}) + (f32x2){bf_lo(xv[n].y), bf_hi(xv[n].y)};
      xv[n] = (u32x2){pk_bf16(lo.x, lo.y), pk_bf16(hi.x, hi.y)};
      if (n & 1) {
        auto rx = __builtin_amdgcn_permlane16_swap(xv[n - 1].x, xv[n].x, false, false);
        auto ry = __builtin_amdgcn_permlane16_swap(xv[n - 1].y, xv[n].y, false, false);
        x2[n >> 1] = (u32x4){rx[0], ry[0], rx[1], ry[1]};
      }
    }
    {
      u32x4 t0, t1;
#pragma unroll
      for (int j = 0; j < 4; ++j) {
        t0[j] = __builtin_amdgcn_update_dpp(x2[0][j], x2[1][j], 0x128, 0xf, 0xc, false);
        t1[j] = __builtin_amdgcn_update_dpp(x2[1][j], x2[0][j], 0x128, 0xf, 0x3, false);
      }
      const unsigned vo = vx - (unsigned)(fr >> 3) * (8 * D_ * 2 - 64);
      __builtin_amdgcn_raw_buffer_store_b128(t0, outr, vo, 0, 0);
      __builtin_amdgcn_raw_buffer_store_b128(t1, outr, vo + 8 * D_ * 2, 0, 0);
    }
  }
  if (bid == 0 && wid == 0) {
    u32x4 f;
    for (unsigned n = 0; n < SPIN_CAP; ++n) {
      asm volatile("global_load_dwordx4 %0, %1, %2 sc1\n\ts_waitcnt vmcnt(0)" : "=&v"(f) : "v"(WS_KVF + (unsigned)(lane * 16)), "s"(ws) : "memory");
      if (__all(f.x == epoch && f.y == epoch && f.z == epoch && f.w == epoch)) break;
      __builtin_amdgcn_s_sleep(2);
    }
    if (lane == 0) __builtin_amdgcn_raw_buffer_store_b32(ev + 1u, wsr, WS_E, 0, 16);
  }
}

extern "C" void kernel_launch(void* const* d_in, const int* in_sizes, int n_in, void* d_out, int out_size, void* d_ws, size_t ws_size,
                              hipStream_t stream) {
  k_fused<<<256, 512, 0, stream>>>((const u16*)d_in[0], (const u16*)d_in[1], (const u16*)d_in[2], (const u16*)d_in[3], (const u16*)d_in[4],
                                   (const u16*)d_in[5], (const u16*)d_in[7], (const u16*)d_in[8], (const u16*)d_in[9], (u16*)d_out,
                                   (char*)d_ws);
}
```
